```python
import torch

def _softmax(x, axis=-1):
    return torch.softmax(x, dim=axis)
def _gelu(x):
    return torch.nn.functional.gelu(x, approximate="tanh")
def _silu(x):
    return torch.nn.functional.silu(x)
def _sigmoid(x):
    return torch.sigmoid(x)
def _layernorm(x, axis=-1, eps=1e-5):
    m = x.mean(dim=axis, keepdim=True)
    v = x.var(dim=axis, keepdim=True, unbiased=False)
    return (x - m) / torch.sqrt(v + eps)
def _scatter_add(tgt, idx, src, axis=0):
    return tgt.clone().index_add_(axis, idx, src)
def _segment_sum(src, seg, n):
    out = torch.zeros((n,) + src.shape[1:], dtype=src.dtype)
    return out.index_add_(0, seg, src)
def _pad(x, pw):
    flat = tuple(v for pair in reversed(pw) for v in pair)
    return torch.nn.functional.pad(x, flat)

kernel_name = 'standalone__window_attn_swin__vision'

def setup_inputs():

    _w = (lambda a: (lambda t: t.to(torch.bfloat16) if t.is_floating_point() else t)(torch.as_tensor(a)))
    H = W = 224
    C = 96
    ws = 7
    nh = 4
    shift = ws // 2
    coords = torch.stack(torch.meshgrid(torch.arange(ws), torch.arange(ws), indexing='ij'), 0).reshape(2, -1)
    rel = coords[:, :, None] - coords[:, None, :]
    rel[0] += ws - 1
    rel[1] += ws - 1
    rel[0] *= 2 * ws - 1
    rp_idx = rel.sum(0).to(torch.int64)
    img_mask = torch.zeros((1, H, W, 1), dtype=float)
    cnt = 0
    for hs in (slice(0, H - ws), slice(H - ws, H - shift), slice(H - shift, H)):
        for vs in (slice(0, W - ws), slice(W - ws, W - shift), slice(W - shift, W)):
            img_mask[:, hs, vs, :] = cnt
            cnt += 1
    mw = img_mask.reshape(1, H // ws, ws, W // ws, ws, 1).permute(0, 1, 3, 2, 4, 5).reshape(-1, ws * ws)
    am = mw[:, :, None] - mw[:, None, :]
    attn_mask = torch.where(am != 0, -100.0, 0.0).to(float)
    return {
        "x": _w((lambda *s: torch.randn(*s, dtype=torch.bfloat16) * 1.6)(1, H, W, C)),
        "wqkv": _w((lambda *s: torch.randn(*s, dtype=torch.bfloat16) * 1.6)(C, 3 * C) * 0.02),
        "wo": _w((lambda *s: torch.randn(*s, dtype=torch.bfloat16) * 0.05)(C, C) * 0.02),
        "rpb": _w((lambda *s: torch.randn(*s, dtype=torch.bfloat16) * 0.4)((2 * ws - 1) * (2 * ws - 1), nh)),
        "rp_idx": _w(rp_idx),
        "attn_mask": _w(attn_mask),
    }

def reference(x, wqkv, wo, rpb, rp_idx, attn_mask):
    B, H, W, C = x.shape
    N = rp_idx.shape[0]
    ws = int(round(N ** 0.5))
    nh = rpb.shape[-1]
    hd = C // nh
    shift = ws // 2
    xs = torch.roll(x, (-shift, -shift), (1, 2))
    nWh = H // ws
    nWw = W // ws
    xw = (lambda x, *d: x.permute(*d))(xs.reshape(B, nWh, ws, nWw, ws, C), 0, 1, 3, 2, 4, 5).reshape(B * nWh * nWw, N, C)
    qkv = (lambda x, *d: x.permute(*d))(torch.einsum('wnc,cd->wnd', xw, wqkv).reshape(-1, N, 3, nh, hd), 2, 0, 3, 1, 4)
    q, k, v = qkv[0], qkv[1], qkv[2]
    attn = torch.einsum('whid,whjd->whij', q, k) * (hd ** -0.5)
    bias = (lambda x, *d: x.permute(*d))(rpb[rp_idx.reshape(-1)].reshape(N, N, nh), 2, 0, 1)
    attn = attn + bias[None, :, :, :]
    attn = (attn.reshape(B, nWh * nWw, nh, N, N) + attn_mask[None, :, None, :, :]).reshape(-1, nh, N, N)
    attn = _softmax(attn, axis=-1)
    out = (lambda x, *d: x.permute(*d))(torch.einsum('whij,whjd->whid', attn, v), 0, 2, 1, 3).reshape(-1, N, C)
    out = torch.einsum('wnc,cd->wnd', out, wo)
    out = (lambda x, *d: x.permute(*d))(out.reshape(B, nWh, nWw, ws, ws, C), 0, 1, 3, 2, 4, 5).reshape(B, H, W, C)
    return torch.roll(out, (shift, shift), (1, 2))
```

```cpp
#include <hip/hip_runtime.h>
#include <hip/hip_bf16.h>
#include <stdio.h>

__device__ __forceinline__ float bf2f(unsigned short v) { return __uint_as_float(((unsigned)v) << 16); }
__device__ __forceinline__ unsigned short f2bf(float x) { unsigned u = __float_as_uint(x); u += 0x7fffu + ((u >> 16) & 1u); return (unsigned short)(u >> 16); }

__global__ __launch_bounds__(256) void naive_kernel(const unsigned short* x, const unsigned short* wqkv, const unsigned short* wo,
    const unsigned short* rpb, const int* rp_idx, const unsigned short* mask, unsigned short* out) {
  __shared__ float xs[49][96];
  __shared__ float qkv[49][288];
  __shared__ float p[49][49];
  __shared__ float o[49][96];
  int w = blockIdx.x; int wh = w / 32, ww = w % 32;
  int t = threadIdx.x;
  if (w == 0 && t == 0) { printf("rp_idx: %d %d %d %d %d %d %d %d\n", rp_idx[0], rp_idx[1], rp_idx[2], rp_idx[3], rp_idx[4], rp_idx[5], rp_idx[2400], rp_idx[2399]); }
  for (int i = t; i < 49 * 96; i += 256) {
    int tok = i / 96, c = i % 96; int r = tok / 7, cc = tok % 7;
    int gy = (wh * 7 + r + 3) % 224, gx = (ww * 7 + cc + 3) % 224;
    xs[tok][c] = bf2f(x[(gy * 224 + gx) * 96 + c]);
  }
  __syncthreads();
  for (int i = t; i < 49 * 288; i += 256) {
    int tok = i / 288, n = i % 288; float s = 0;
    for (int c = 0; c < 96; ++c) s += xs[tok][c] * bf2f(wqkv[c * 288 + n]);
    qkv[tok][n] = bf2f(f2bf(s));
  }
  __syncthreads();
  for (int h = 0; h < 4; ++h) {
    for (int i = t; i < 49 * 49; i += 256) {
      int a = i / 49, b = i % 49; float s = 0;
      for (int d = 0; d < 24; ++d) s += qkv[a][h * 24 + d] * qkv[b][96 + h * 24 + d];
      s *= 0.20412414523193154f;
      s += bf2f(rpb[rp_idx[i] * 4 + h]);
      s += bf2f(mask[w * 2401 + i]);
      p[a][b] = s;
    }
    __syncthreads();
    if (t < 49) {
      float m = -1e30f; for (int b = 0; b < 49; ++b) m = fmaxf(m, p[t][b]);
      float l = 0; for (int b = 0; b < 49; ++b) { float e = __expf(p[t][b] - m); p[t][b] = e; l += e; }
      float r = 1.f / l; for (int b = 0; b < 49; ++b) p[t][b] *= r;
    }
    __syncthreads();
    for (int i = t; i < 49 * 24; i += 256) {
      int a = i / 24, d = i % 24; float s = 0;
      for (int b = 0; b < 49; ++b) s += p[a][b] * qkv[b][192 + h * 24 + d];
      o[a][h * 24 + d] = s;
    }
    __syncthreads();
  }
  for (int i = t; i < 49 * 96; i += 256) {
    int tok = i / 96, n = i % 96; float s = 0;
    for (int c = 0; c < 96; ++c) s += o[tok][c] * bf2f(wo[c * 96 + n]);
    int r = tok / 7, cc = tok % 7;
    int gy = (wh * 7 + r + 3) % 224, gx = (ww * 7 + cc + 3) % 224;
    out[(gy * 224 + gx) * 96 + n] = f2bf(s);
  }
}

extern "C" void kernel_launch(void* const* d_in, const int* in_sizes, int n_in,
                              void* d_out, int out_size, void* d_ws, size_t ws_size,
                              hipStream_t stream) {
  naive_kernel<<<1024, 256, 0, stream>>>((const unsigned short*)d_in[0], (const unsigned short*)d_in[1], (const unsigned short*)d_in[2],
     (const unsigned short*)d_in[3], (const int*)d_in[4], (const unsigned short*)d_in[5], (unsigned short*)d_out);
}
```

```cpp
#include <hip/hip_runtime.h>
#include <stdint.h>

typedef float f32x16 __attribute__((ext_vector_type(16)));
typedef float f32x2 __attribute__((ext_vector_type(2)));
typedef __bf16 bf16x8 __attribute__((ext_vector_type(8)));
typedef __bf16 bf16x2 __attribute__((ext_vector_type(2)));
typedef short s16x4 __attribute__((ext_vector_type(4)));
typedef unsigned u32x4 __attribute__((ext_vector_type(4)));
typedef unsigned u32x2 __attribute__((ext_vector_type(2)));

#if defined(__HIP_DEVICE_COMPILE__)
#define NOPK __attribute__((target("no-packed-fp32-ops")))
#else
#define NOPK
#endif
#define DI __device__ __forceinline__ NOPK
#define LDSP(T, a) ((__attribute__((address_space(3))) T*)(a))

constexpr int LDS_W = 0;
constexpr int W_IMG = 96 * 320;
constexpr int LDS_WO = 2 * W_IMG;
constexpr int LDS_X = LDS_WO + 18432;
constexpr int X_WIN = 49 * 192;
constexpr int LDS_M = LDS_X + 4 * X_WIN;
constexpr int LDS_B = LDS_M + 1201 * 16;
constexpr int LDS_RPB = LDS_B + 4 * 49 * 112;
constexpr int LDS_TOTAL = LDS_RPB + 1360;

DI unsigned cvtpk(float lo, float hi) {
  f32x2 v = {lo, hi};
  bf16x2 b = __builtin_convertvector(v, bf16x2);
  return __builtin_bit_cast(unsigned, b);
}
DI float max3f(float a, float b, float c) {
  float r;
  asm("v_max3_f32 %0, %1, %2, %3" : "=v"(r) : "v"(a), "v"(b), "v"(c));
  return r;
}
DI float max2f(float a, float b) {
  float r;
  asm("v_max_f32_e32 %0, %1, %2" : "=v"(r) : "v"(a), "v"(b));
  return r;
}
DI bf16x8 mk8(unsigned a, unsigned b, unsigned c, unsigned d) {
  u32x4 v = {a, b, c, d};
  return __builtin_bit_cast(bf16x8, v);
}
DI bf16x8 pack8(const f32x16& a, int r0) {
  return mk8(cvtpk(a[r0], a[r0 + 1]), cvtpk(a[r0 + 2], a[r0 + 3]), cvtpk(a[r0 + 4], a[r0 + 5]), cvtpk(a[r0 + 6], a[r0 + 7]));
}
DI f32x16 mfma(bf16x8 a, bf16x8 b, f32x16 c) { return __builtin_amdgcn_mfma_f32_32x32x16_bf16(a, b, c, 0, 0, 0); }

DI bf16x8 tr2(unsigned addr0, unsigned addr1) {
  s16x4 lo = __builtin_amdgcn_ds_read_tr16_b64_v4i16(LDSP(s16x4, addr0));
  s16x4 hi = __builtin_amdgcn_ds_read_tr16_b64_v4i16(LDSP(s16x4, addr1));
  u32x2 a = __builtin_bit_cast(u32x2, lo), b = __builtin_bit_cast(u32x2, hi);
  return mk8(a[0], a[1], b[0], b[1]);
}
DI bf16x8 ldsr128(unsigned addr) { return *LDSP(bf16x8, addr); }

DI void dma16(const void* g, unsigned ldsaddr) {
  __builtin_amdgcn_global_load_lds((const __attribute__((address_space(1))) void*)g, LDSP(void, ldsaddr), 16, 0, 0);
}

__global__ __launch_bounds__(512, 2) NOPK void swin_attn_kernel(
    const unsigned short* __restrict__ x, const unsigned short* __restrict__ wqkv, const unsigned short* __restrict__ wo,
    const unsigned short* __restrict__ rpb, const int* __restrict__ rp_idx, const unsigned short* __restrict__ mask,
    unsigned short* __restrict__ out) {
  __shared__ __attribute__((aligned(16))) char smem[LDS_TOTAL];
  const unsigned sb = (unsigned)(uintptr_t)LDSP(char, smem);
  const int tid = threadIdx.x;
  const int lane = tid & 63;
  const int wv = __builtin_amdgcn_readfirstlane(tid >> 6);
  const int b = blockIdx.x;
  const int wh = b >> 3, ww0 = (b & 7) * 4;
  const int h = lane >> 5, l31 = lane & 31;

  {
    const int r3 = lane / 20, ch = lane - r3 * 20;
    const unsigned long long T0 = 0ull | (0ull << 0) | (1ull << 5) | (3ull << 10) | (4ull << 15) | (12ull << 20) | (13ull << 25) | (15ull << 30) |
                                  (16ull << 35) | (2ull << 40) | (5ull << 45) | (14ull << 50) | (17ull << 55);
    const unsigned long long T1 = (24ull << 0) | (25ull << 5) | (26ull << 10) | (0ull << 15) | (27ull << 20) | (28ull << 25) | (29ull << 30);
    const unsigned gam = (unsigned)((ch < 12 ? (T0 >> (5 * ch)) : (T1 >> (5 * (ch - 12)))) & 31ull);
    const int P = wv & 1, rb = (wv >> 1) * 24;
    const char* src = (const char*)wqkv + (rb + r3) * 576 + (gam + 6 * P) * 16;
    if (lane < 60 && ch != 15 && ch != 19) {
#pragma unroll
      for (int j = 0; j < 8; ++j) dma16(src + j * 3 * 576, sb + LDS_W + P * W_IMG + (rb + 3 * j) * 320);
    }
    if (tid < 384) {
      const int row = tid >> 2, im = (tid >> 1) & 1, z = tid & 1;
      *LDSP(u32x4, sb + LDS_W + im * W_IMG + row * 320 + (15 + 4 * z) * 16) = u32x4{0, 0, 0, 0};
    }
  }
  {
    const int ybase = wh * 7 + 3;
#pragma unroll
    for (int pass = 0; pass < 2; ++pass) {
      const int j = pass == 0 ? wv : 8 + (wv & 1);
      const int i = 64 * j + lane;
      const int tok = i / 12, r = i / 84, cc = tok - 7 * r;
      const int c = (i - 12 * tok) ^ ((tok >> 2) & 3);
      int gy = ybase + r;
      gy = gy >= 224 ? gy - 224 : gy;
      if (pass == 0) {
#pragma unroll
        for (int w = 0; w < 4; ++w) {
          int gx = (ww0 + w) * 7 + 3 + cc;
          gx = gx >= 224 ? gx - 224 : gx;
          dma16((const char*)x + ((gy * 224 + gx) * 192 + c * 16), sb + LDS_X + w * X_WIN + j * 1024);
        }
      } else {
        const int w = wv >> 1;
        int gx = (ww0 + w) * 7 + 3 + cc;
        gx = gx >= 224 ? gx - 224 : gx;
        if (i < 588) dma16((const char*)x + ((gy * 224 + gx) * 192 + c * 16), sb + LDS_X + w * X_WIN + j * 1024);
      }
    }
  }
  int ia[3], ib[3], i48 = 0;
  {
#pragma unroll
    for (int rd = 0; rd < 3; ++rd) {
      const int e = tid + 512 * rd;
      const int q = e / 24, kp = e - 24 * q;
      ia[rd] = 0; ib[rd] = 0;
      if (e < 1176) { ia[rd] = rp_idx[q * 49 + 2 * kp]; ib[rd] = rp_idx[q * 49 + 2 * kp + 1]; }
    }
    if (tid < 49) i48 = rp_idx[tid * 49 + 48];
    if (tid >= 256 && tid < 256 + 169) {
      const int t = tid - 256;
      u32x2 v = *(const u32x2*)(rpb + t * 4);
      *LDSP(u32x2, sb + LDS_RPB + t * 8) = v;
    }
  }
  {
    const unsigned moff = ((unsigned)b * 19208u) & ~15u;
#pragma unroll
    for (int it = 0; it < 3; ++it) {
      const int j = wv + 8 * it;
      const int i = 64 * j + lane;
      if (i < 1201) dma16((const char*)mask + moff + i * 16, sb + LDS_M + j * 1024);
    }
#pragma unroll
    for (int it = 0; it < 3; ++it) {
      const int j = wv + 8 * it;
      if (j < 18) dma16((const char*)wo + (64 * j + lane) * 16, sb + LDS_WO + j * 1024);
    }
  }
  asm volatile("s_waitcnt vmcnt(0) lgkmcnt(0)" ::: "memory");
  __builtin_amdgcn_s_barrier();

  {
#pragma unroll
    for (int rd = 0; rd < 3; ++rd) {
      const int e = tid + 512 * rd;
      const int q = e / 24, kp = e - 24 * q;
      if (e < 1176) {
        const u32x2 va = *LDSP(u32x2, sb + LDS_RPB + ia[rd] * 8);
        const u32x2 vb = *LDSP(u32x2, sb + LDS_RPB + ib[rd] * 8);
        const unsigned dst = sb + LDS_B + q * 112 + kp * 4;
        *LDSP(unsigned, dst + 0 * 5488) = __builtin_amdgcn_perm(vb[0], va[0], 0x05040100u);
        *LDSP(unsigned, dst + 1 * 5488) = __builtin_amdgcn_perm(vb[0], va[0], 0x07060302u);
        *LDSP(unsigned, dst + 2 * 5488) = __builtin_amdgcn_perm(vb[1], va[1], 0x05040100u);
        *LDSP(unsigned, dst + 3 * 5488) = __builtin_amdgcn_perm(vb[1], va[1], 0x07060302u);
      }
    }
    if (tid < 49) {
      const u32x2 va = *LDSP(u32x2, sb + LDS_RPB + i48 * 8);
      const unsigned dst = sb + LDS_B + tid * 112 + 96;
      *LDSP(u32x2, dst + 0 * 5488) = u32x2{va[0] << 16, 0xff800000u};
      *LDSP(u32x2, dst + 1 * 5488) = u32x2{va[0] & 0xffff0000u, 0xff800000u};
      *LDSP(u32x2, dst + 2 * 5488) = u32x2{va[1] << 16, 0xff800000u};
      *LDSP(u32x2, dst + 3 * 5488) = u32x2{va[1] & 0xffff0000u, 0xff800000u};
    }
  }

  const int wi = wv >> 1, P = wv & 1;
  bf16x8 xf[2][6];
  {
#pragma unroll
    for (int u = 0; u < 2; ++u) {
      int tok = 32 * u + l31;
      tok = tok > 48 ? 48 : tok;
      const unsigned base = sb + LDS_X + wi * X_WIN + tok * 192;
      const unsigned sw = (unsigned)(h ^ ((tok >> 2) & 3));
      const unsigned aE = base + sw * 16, aO = base + (sw ^ 2) * 16;
#pragma unroll
      for (int s = 0; s < 6; ++s) xf[u][s] = ldsr128(((s & 1) ? aO : aE) + (s >> 1) * 64);
    }
  }
  const int li = lane & 15, tq = li >> 2, tp = li & 3, g1 = (lane >> 4) & 1;
  const unsigned wbase = sb + LDS_W + P * W_IMG + (8 * h + tq) * 320 + g1 * 32 + tp * 8;

  bf16x8 qf01[2][2], qf2[2][2];
  bf16x8 kf01[2][2], kf22[2];
  bf16x8 vf[2][4];
  const float qscale = 0.20412414523193154f;
  const f32x16 zero16 = {0, 0, 0, 0, 0, 0, 0, 0, 0, 0, 0, 0, 0, 0, 0, 0};
  {
#pragma unroll
    for (int T = 0; T < 3; ++T) {
      f32x16 a0 = zero16, a1 = zero16;
#pragma unroll
      for (int s = 0; s < 6; ++s) {
        const bf16x8 wf = tr2(wbase + (16 * s) * 320 + T * 64, wbase + (16 * s + 4) * 320 + T * 64);
        a0 = mfma(wf, xf[0][s], a0);
        a1 = mfma(wf, xf[1][s], a1);
      }
      if (T == 0) {
#pragma unroll
        for (int r = 0; r < 16; ++r) { a0[r] *= qscale; a1[r] *= qscale; }
        qf01[0][0] = pack8(a0, 0); qf01[0][1] = pack8(a0, 8);
        qf01[1][0] = pack8(a1, 0); qf01[1][1] = pack8(a1, 8);
      } else if (T == 1) {
        kf01[0][0] = pack8(a0, 0); kf01[0][1] = pack8(a0, 8);
        kf01[1][0] = pack8(a1, 0); kf01[1][1] = pack8(a1, 8);
      } else {
#pragma unroll
        for (int r = 0; r < 8; ++r) { a0[r] *= qscale; a1[r] *= qscale; }
        qf2[0][0] = mk8(cvtpk(a0[0], a0[1]), cvtpk(a0[2], a0[3]), 0u, 0u);
        qf2[0][1] = mk8(0u, 0u, cvtpk(a0[4], a0[5]), cvtpk(a0[6], a0[7]));
        qf2[1][0] = mk8(cvtpk(a1[0], a1[1]), cvtpk(a1[2], a1[3]), 0u, 0u);
        qf2[1][1] = mk8(0u, 0u, cvtpk(a1[4], a1[5]), cvtpk(a1[6], a1[7]));
        kf22[0] = pack8(a0, 8);
        kf22[1] = pack8(a1, 8);
      }
    }
    f32x16 cone;
    {
      const float cv = l31 >= 24 ? 1.0f : 0.0f;
#pragma unroll
      for (int r = 0; r < 16; ++r) cone[r] = cv;
    }
#pragma unroll
    for (int hd = 0; hd < 2; ++hd) {
      f32x16 a0 = cone, a1 = cone;
#pragma unroll
      for (int s = 0; s < 6; ++s) {
        const bf16x8 wf = tr2(wbase + (16 * s) * 320 + (3 + hd) * 64, wbase + (16 * s + 4) * 320 + (3 + hd) * 64);
        a0 = mfma(xf[0][s], wf, a0);
        a1 = mfma(xf[1][s], wf, a1);
      }
      vf[hd][0] = pack8(a0, 0);
      vf[hd][1] = pack8(a0, 8);
      vf[hd][2] = pack8(a1, 0);
      vf[hd][3] = mk8(cvtpk(a1[8], a1[9]), 0u, 0u, 0u);
    }
  }
  asm volatile("s_waitcnt lgkmcnt(0)" ::: "memory");
  __builtin_amdgcn_s_barrier();

  bf16x8 id0, id1;
  {
    const int d0 = l31 - 8 * h;
    const int d1 = l31 - 16 - 8 * h;
    unsigned a[4], c[4];
#pragma unroll
    for (int k = 0; k < 4; ++k) {
      a[k] = (d0 == 2 * k ? 0x3f80u : 0u) | (d0 == 2 * k + 1 ? 0x3f800000u : 0u);
      c[k] = (d1 == 2 * k ? 0x3f80u : 0u) | (d1 == 2 * k + 1 ? 0x3f800000u : 0u);
    }
    id0 = mk8(a[0], a[1], a[2], a[3]);
    id1 = mk8(c[0], c[1], c[2], c[3]);
  }
  const unsigned mdelta = ((unsigned)b * 19208u) & 15u;
  const float LOG2E = 1.4426950408889634f;
  bf16x8 of[2][3];
#pragma unroll
  for (int uu = 0; uu < 2; ++uu) {
    const int u = uu ^ 1 ^ P;
    int q = 32 * u + l31;
    q = q > 48 ? 48 : q;
    f32x16 m0, m1 = zero16;
    {
      const unsigned mb48 = sb + LDS_M + mdelta + wi * 4802 + q * 98;
      const unsigned mb = mb48 + 8 * h;
      float t[25];
      asm volatile(
          "ds_read_u16_d16_hi %0, %25 offset:0\n ds_read_u16_d16_hi %1, %25 offset:2\n ds_read_u16_d16_hi %2, %25 offset:4\n ds_read_u16_d16_hi %3, %25 offset:6\n"
          "ds_read_u16_d16_hi %4, %25 offset:16\n ds_read_u16_d16_hi %5, %25 offset:18\n ds_read_u16_d16_hi %6, %25 offset:20\n ds_read_u16_d16_hi %7, %25 offset:22\n"
          "ds_read_u16_d16_hi %8, %25 offset:32\n ds_read_u16_d16_hi %9, %25 offset:34\n ds_read_u16_d16_hi %10, %25 offset:36\n ds_read_u16_d16_hi %11, %25 offset:38\n"
          "ds_read_u16_d16_hi %12, %25 offset:48\n ds_read_u16_d16_hi %13, %25 offset:50\n ds_read_u16_d16_hi %14, %25 offset:52\n ds_read_u16_d16_hi %15, %25 offset:54\n"
          "ds_read_u16_d16_hi %16, %25 offset:64\n ds_read_u16_d16_hi %17, %25 offset:66\n ds_read_u16_d16_hi %18, %25 offset:68\n ds_read_u16_d16_hi %19, %25 offset:70\n"
          "ds_read_u16_d16_hi %20, %25 offset:80\n ds_read_u16_d16_hi %21, %25 offset:82\n ds_read_u16_d16_hi %22, %25 offset:84\n ds_read_u16_d16_hi %23, %25 offset:86\n"
          "ds_read_u16_d16_hi %24, %26 offset:96\n s_waitcnt lgkmcnt(0)\n"
          : "=&v"(t[0]), "=&v"(t[1]), "=&v"(t[2]), "=&v"(t[3]), "=&v"(t[4]), "=&v"(t[5]), "=&v"(t[6]), "=&v"(t[7]), "=&v"(t[8]), "=&v"(t[9]),
            "=&v"(t[10]), "=&v"(t[11]), "=&v"(t[12]), "=&v"(t[13]), "=&v"(t[14]), "=&v"(t[15]), "=&v"(t[16]), "=&v"(t[17]), "=&v"(t[18]),
            "=&v"(t[19]), "=&v"(t[20]), "=&v"(t[21]), "=&v"(t[22]), "=&v"(t[23]), "=&v"(t[24])
          : "v"(mb), "v"(mb48)
          : "memory");
#pragma unroll
      for (int r = 0; r < 16; ++r) m0[r] = t[r];
#pragma unroll
      for (int r = 0; r < 9; ++r) m1[r] = t[16 + r];
    }
    unsigned og[2][6];
#pragma unroll
    for (int hd = 0; hd < 2; ++hd) {
      const unsigned bb = sb + LDS_B + (2 * P + hd) * 5488 + q * 112;
      const bf16x8 b0 = ldsr128(bb + 16 * h), b1 = ldsr128(bb + 32 + 16 * h), b2 = ldsr128(bb + 64 + 16 * h);
      const float b48 = *LDSP(float, bb + 96 + 4 * h);
      const bf16x8 qa = u ? qf01[1][hd] : qf01[0][hd];
      const bf16x8 qb = u ? qf2[1][hd] : qf2[0][hd];
      f32x16 s0 = mfma(kf01[0][hd], qa, m0);
      f32x16 s1 = mfma(kf01[1][hd], qa, m1);
      s0 = mfma(kf22[0], qb, s0);
      s1 = mfma(kf22[1], qb, s1);
      s0 = mfma(id0, b0, s0);
      s1 = mfma(id0, b2, s1);
      s0 = mfma(id1, b1, s0);
      const float s48 = s1[8] + b48;
      float ma = max3f(s0[0], s0[1], s0[2]), mb_ = max3f(s0[3], s0[4], s0[5]), mc = max3f(s0[6], s0[7], s0[8]);
      ma = max3f(ma, s0[9], s0[10]); mb_ = max3f(mb_, s0[11], s0[12]); mc = max3f(mc, s0[13], s0[14]);
      ma = max3f(ma, s0[15], s1[0]); mb_ = max3f(mb_, s1[1], s1[2]); mc = max3f(mc, s1[3], s1[4]);
      ma = max3f(ma, s1[5], s1[6]); mb_ = max3f(mb_, s1[7], s48);
      float mx = max3f(ma, mb_, mc);
      {
        const unsigned mu = __builtin_bit_cast(unsigned, mx);
        auto sw = __builtin_amdgcn_permlane32_swap(mu, mu, false, false);
        mx = max2f(__builtin_bit_cast(float, sw[0]), __builtin_bit_cast(float, sw[1]));
      }
      const float mneg = -mx * LOG2E;
      float p0[16], p1[9];
#pragma unroll
      for (int r = 0; r < 16; ++r) p0[r] = __builtin_amdgcn_exp2f(__builtin_fmaf(s0[r], LOG2E, mneg));
#pragma unroll
      for (int r = 0; r < 8; ++r) p1[r] = __builtin_amdgcn_exp2f(__builtin_fmaf(s1[r], LOG2E, mneg));
      p1[8] = __builtin_amdgcn_exp2f(__builtin_fmaf(s48, LOG2E, mneg));
      const bf16x8 pf0 = mk8(cvtpk(p0[0], p0[1]), cvtpk(p0[2], p0[3]), cvtpk(p0[4], p0[5]), cvtpk(p0[6], p0[7]));
      const bf16x8 pf1 = mk8(cvtpk(p0[8], p0[9]), cvtpk(p0[10], p0[11]), cvtpk(p0[12], p0[13]), cvtpk(p0[14], p0[15]));
      const bf16x8 pf2 = mk8(cvtpk(p1[0], p1[1]), cvtpk(p1[2], p1[3]), cvtpk(p1[4], p1[5]), cvtpk(p1[6], p1[7]));
      const bf16x8 pf3 = mk8(cvtpk(p1[8], 0.0f), 0u, 0u, 0u);
      f32x16 o = mfma(vf[hd][0], pf0, zero16);
      o = mfma(vf[hd][1], pf1, o);
      o = mfma(vf[hd][2], pf2, o);
      o = mfma(vf[hd][3], pf3, o);
      const float inv = __builtin_amdgcn_rcpf(o[12]);
#pragma unroll
      for (int k = 0; k < 6; ++k) og[hd][k] = cvtpk(o[2 * k] * inv, o[2 * k + 1] * inv);
    }
    const bf16x8 f0 = mk8(og[0][0], og[0][1], og[0][2], og[0][3]);
    const bf16x8 f1 = mk8(og[0][4], og[0][5], og[1][0], og[1][1]);
    const bf16x8 f2 = mk8(og[1][2], og[1][3], og[1][4], og[1][5]);
    if (uu == 0) {
      const unsigned xa = sb + LDS_X + wi * X_WIN + P * 3072 + lane * 16;
      *LDSP(bf16x8, xa) = f0;
      *LDSP(bf16x8, xa + 1024) = f1;
      *LDSP(bf16x8, xa + 2048) = f2;
    } else {
      of[0][0] = f0; of[0][1] = f1; of[0][2] = f2;
    }
  }
  asm volatile("s_waitcnt lgkmcnt(0)" ::: "memory");
  __builtin_amdgcn_s_barrier();
  {
    const unsigned xa = sb + LDS_X + wi * X_WIN + (P ^ 1) * 3072 + lane * 16;
    of[1][0] = ldsr128(xa);
    of[1][1] = ldsr128(xa + 1024);
    of[1][2] = ldsr128(xa + 2048);
  }
  {
    const unsigned wob = sb + LDS_WO + (4 * h + tq) * 192 + (16 * g1 + 8 * (tp & 1) + 4 * (tp >> 1)) * 2;
    f32x16 acc[3] = {zero16, zero16, zero16};
#pragma unroll
    for (int s = 0; s < 6; ++s) {
      const int ss = s % 3;
      const bf16x8 own = of[0][ss], oth = of[1][ss];
      const bool mine = (s / 3) == P;
      const bf16x8 bfr = mine ? own : oth;
#pragma unroll
      for (int T = 0; T < 3; ++T) {
        const bf16x8 wf = tr2(wob + (16 * s) * 192 + 64 * T, wob + (16 * s + 8) * 192 + 64 * T);
        acc[T] = mfma(wf, bfr, acc[T]);
      }
    }
    const int tok = 32 * P + l31;
    if (tok < 49) {
      const int r = tok / 7, cc = tok - 7 * r;
      int gy = wh * 7 + 3 + r;
      gy = gy >= 224 ? gy - 224 : gy;
      int gx = (ww0 + wi) * 7 + 3 + cc;
      gx = gx >= 224 ? gx - 224 : gx;
      char* dst = (char*)out + (gy * 224 + gx) * 192 + 16 * h;
#pragma unroll
      for (int T = 0; T < 3; ++T) {
        *(bf16x8*)(dst + 64 * T) = pack8(acc[T], 0);
        *(bf16x8*)(dst + 64 * T + 32) = pack8(acc[T], 8);
      }
    }
  }
}

extern "C" void kernel_launch(void* const* d_in, const int* in_sizes, int n_in, void* d_out, int out_size, void* d_ws,
                              size_t ws_size, hipStream_t stream) {
  swin_attn_kernel<<<256, 512, 0, stream>>>((const unsigned short*)d_in[0], (const unsigned short*)d_in[1],
                                            (const unsigned short*)d_in[2], (const unsigned short*)d_in[3],
                                            (const int*)d_in[4], (const unsigned short*)d_in[5], (unsigned short*)d_out);
}
```

```cpp
#include <hip/hip_runtime.h>
#include <stdint.h>

typedef float f32x16 __attribute__((ext_vector_type(16)));
typedef float f32x2 __attribute__((ext_vector_type(2)));
typedef __bf16 bf16x8 __attribute__((ext_vector_type(8)));
typedef __bf16 bf16x2 __attribute__((ext_vector_type(2)));
typedef short s16x4 __attribute__((ext_vector_type(4)));
typedef unsigned u32x4 __attribute__((ext_vector_type(4)));
typedef unsigned u32x2 __attribute__((ext_vector_type(2)));

#if defined(__HIP_DEVICE_COMPILE__)
#define NOPK __attribute__((target("no-packed-fp32-ops")))
#else
#define NOPK
#endif
#define DI __device__ __forceinline__ NOPK
#define LDSP(T, a) ((__attribute__((address_space(3))) T*)(a))

constexpr int LDS_W = 0;
constexpr int W_IMG = 96 * 320;
constexpr int LDS_WO = 2 * W_IMG;
constexpr int LDS_X = LDS_WO + 18432;
constexpr int X_WIN = 49 * 192;
constexpr int LDS_M = LDS_X + 4 * X_WIN;
constexpr int LDS_B = LDS_M + 1201 * 16;
constexpr int LDS_RPB = LDS_B + 4 * 49 * 112;
constexpr int LDS_TOTAL = LDS_RPB + 1360;

DI unsigned cvtpk(float lo, float hi) {
  f32x2 v = {lo, hi};
  bf16x2 b = __builtin_convertvector(v, bf16x2);
  return __builtin_bit_cast(unsigned, b);
}
DI float max3f(float a, float b, float c) {
  float r;
  asm("v_max3_f32 %0, %1, %2, %3" : "=v"(r) : "v"(a), "v"(b), "v"(c));
  return r;
}
DI float max2f(float a, float b) {
  float r;
  asm("v_max_f32_e32 %0, %1, %2" : "=v"(r) : "v"(a), "v"(b));
  return r;
}
DI bf16x8 mk8(unsigned a, unsigned b, unsigned c, unsigned d) {
  u32x4 v = {a, b, c, d};
  return __builtin_bit_cast(bf16x8, v);
}
DI bf16x8 pack8(const f32x16& a, int r0) {
  return mk8(cvtpk(a[r0], a[r0 + 1]), cvtpk(a[r0 + 2], a[r0 + 3]), cvtpk(a[r0 + 4], a[r0 + 5]), cvtpk(a[r0 + 6], a[r0 + 7]));
}
DI f32x16 mfma(bf16x8 a, bf16x8 b, f32x16 c) { return __builtin_amdgcn_mfma_f32_32x32x16_bf16(a, b, c, 0, 0, 0); }

DI bf16x8 tr2(unsigned addr0, unsigned addr1) {
  s16x4 lo = __builtin_amdgcn_ds_read_tr16_b64_v4i16(LDSP(s16x4, addr0));
  s16x4 hi = __builtin_amdgcn_ds_read_tr16_b64_v4i16(LDSP(s16x4, addr1));
  u32x2 a = __builtin_bit_cast(u32x2, lo), b = __builtin_bit_cast(u32x2, hi);
  return mk8(a[0], a[1], b[0], b[1]);
}
DI bf16x8 ldsr128(unsigned addr) { return *LDSP(bf16x8, addr); }

#define STAMP(k)
DI void dma16(const void* g, unsigned ldsaddr) {
  __builtin_amdgcn_global_load_lds((const __attribute__((address_space(1))) void*)g, LDSP(void, ldsaddr), 16, 0, 0);
}

__global__ __launch_bounds__(512, 2) NOPK void swin_attn_kernel(
    const unsigned short* __restrict__ x, const unsigned short* __restrict__ wqkv, const unsigned short* __restrict__ wo,
    const unsigned short* __restrict__ rpb, const int* __restrict__ rp_idx, const unsigned short* __restrict__ mask,
    unsigned short* __restrict__ out
    ) {
  __shared__ __attribute__((aligned(16))) char smem[LDS_TOTAL];
  const unsigned sb = (unsigned)(uintptr_t)LDSP(char, smem);
  const int tid = threadIdx.x;
  const int lane = tid & 63;
  const int wv = __builtin_amdgcn_readfirstlane(tid >> 6);
  const int b = blockIdx.x;
  const int wh = b >> 3, ww0 = (b & 7) * 4;
  const int h = lane >> 5, l31 = lane & 31;

  {
    const int r3 = lane / 20, ch = lane - r3 * 20;
    const unsigned long long T0 = 0ull | (0ull << 0) | (1ull << 5) | (3ull << 10) | (4ull << 15) | (12ull << 20) | (13ull << 25) | (15ull << 30) |
                                  (16ull << 35) | (2ull << 40) | (5ull << 45) | (14ull << 50) | (17ull << 55);
    const unsigned long long T1 = (24ull << 0) | (25ull << 5) | (26ull << 10) | (0ull << 15) | (27ull << 20) | (28ull << 25) | (29ull << 30);
    const unsigned gam = (unsigned)((ch < 12 ? (T0 >> (5 * ch)) : (T1 >> (5 * (ch - 12)))) & 31ull);
    const int P = wv & 1, rb = (wv >> 1) * 24;
    const char* src = (const char*)wqkv + (rb + r3) * 576 + (gam + 6 * P) * 16;
    if (lane < 60 && ch != 15 && ch != 19) {
#pragma unroll
      for (int j = 0; j < 8; ++j) dma16(src + j * 3 * 576, sb + LDS_W + P * W_IMG + (rb + 3 * j) * 320);
    }
    if (tid < 384) {
      const int row = tid >> 2, im = (tid >> 1) & 1, z = tid & 1;
      *LDSP(u32x4, sb + LDS_W + im * W_IMG + row * 320 + (15 + 4 * z) * 16) = u32x4{0, 0, 0, 0};
    }
  }
  {
    const int ybase = wh * 7 + 3;
#pragma unroll
    for (int pass = 0; pass < 2; ++pass) {
      const int j = pass == 0 ? wv : 8 + (wv & 1);
      const int i = 64 * j + lane;
      const int tok = i / 12, r = i / 84, cc = tok - 7 * r;
      const int c = (i - 12 * tok) ^ ((tok >> 2) & 3);
      int gy = ybase + r;
      gy = gy >= 224 ? gy - 224 : gy;
      if (pass == 0) {
#pragma unroll
        for (int w = 0; w < 4; ++w) {
          int gx = (ww0 + w) * 7 + 3 + cc;
          gx = gx >= 224 ? gx - 224 : gx;
          dma16((const char*)x + ((gy * 224 + gx) * 192 + c * 16), sb + LDS_X + w * X_WIN + j * 1024);
        }
      } else {
        const int w = wv >> 1;
        int gx = (ww0 + w) * 7 + 3 + cc;
        gx = gx >= 224 ? gx - 224 : gx;
        if (i < 588) dma16((const char*)x + ((gy * 224 + gx) * 192 + c * 16), sb + LDS_X + w * X_WIN + j * 1024);
      }
    }
  }
  int ia[3], ib[3], i48 = 0;
  {
#pragma unroll
    for (int rd = 0; rd < 3; ++rd) {
      const int e = tid + 512 * rd;
      const int q = e / 24, kp = e - 24 * q;
      ia[rd] = 0; ib[rd] = 0;
      if (e < 1176) { ia[rd] = rp_idx[q * 49 + 2 * kp]; ib[rd] = rp_idx[q * 49 + 2 * kp + 1]; }
    }
    if (tid < 49) i48 = rp_idx[tid * 49 + 48];
    if (tid >= 256 && tid < 256 + 169) {
      const int t = tid - 256;
      u32x2 v = *(const u32x2*)(rpb + t * 4);
      *LDSP(u32x2, sb + LDS_RPB + t * 8) = v;
    }
  }
  {
    const unsigned moff = ((unsigned)b * 19208u) & ~15u;
#pragma unroll
    for (int it = 0; it < 3; ++it) {
      const int j = wv + 8 * it;
      const int i = 64 * j + lane;
      if (i < 1201) dma16((const char*)mask + moff + i * 16, sb + LDS_M + j * 1024);
    }
#pragma unroll
    for (int it = 0; it < 3; ++it) {
      const int j = wv + 8 * it;
      if (j < 18) dma16((const char*)wo + (64 * j + lane) * 16, sb + LDS_WO + j * 1024);
    }
  }
  STAMP(2);
  asm volatile("s_waitcnt vmcnt(0) lgkmcnt(0)" ::: "memory");
  STAMP(3);
  __builtin_amdgcn_s_barrier();
  STAMP(4);

  {
#pragma unroll
    for (int rd = 0; rd < 3; ++rd) {
      const int e = tid + 512 * rd;
      const int q = e / 24, kp = e - 24 * q;
      if (e < 1176) {
        const u32x2 va = *LDSP(u32x2, sb + LDS_RPB + ia[rd] * 8);
        const u32x2 vb = *LDSP(u32x2, sb + LDS_RPB + ib[rd] * 8);
        const unsigned dst = sb + LDS_B + q * 112 + kp * 4;
        *LDSP(unsigned, dst + 0 * 5488) = __builtin_amdgcn_perm(vb[0], va[0], 0x05040100u);
        *LDSP(unsigned, dst + 1 * 5488) = __builtin_amdgcn_perm(vb[0], va[0], 0x07060302u);
        *LDSP(unsigned, dst + 2 * 5488) = __builtin_amdgcn_perm(vb[1], va[1], 0x05040100u);
        *LDSP(unsigned, dst + 3 * 5488) = __builtin_amdgcn_perm(vb[1], va[1], 0x07060302u);
      }
    }
    if (tid < 49) {
      const u32x2 va = *LDSP(u32x2, sb + LDS_RPB + i48 * 8);
      const unsigned dst = sb + LDS_B + tid * 112 + 96;
      *LDSP(u32x2, dst + 0 * 5488) = u32x2{va[0] << 16, 0xff800000u};
      *LDSP(u32x2, dst + 1 * 5488) = u32x2{va[0] & 0xffff0000u, 0xff800000u};
      *LDSP(u32x2, dst + 2 * 5488) = u32x2{va[1] << 16, 0xff800000u};
      *LDSP(u32x2, dst + 3 * 5488) = u32x2{va[1] & 0xffff0000u, 0xff800000u};
    }
  }

  STAMP(5);
  const int wi = wv >> 1, P = wv & 1;
  bf16x8 xf[2][6];
  {
#pragma unroll
    for (int u = 0; u < 2; ++u) {
      int tok = 32 * u + l31;
      tok = tok > 48 ? 48 : tok;
      const unsigned base = sb + LDS_X + wi * X_WIN + tok * 192;
      const unsigned sw = (unsigned)(h ^ ((tok >> 2) & 3));
      const unsigned aE = base + sw * 16, aO = base + (sw ^ 2) * 16;
#pragma unroll
      for (int s = 0; s < 6; ++s) xf[u][s] = ldsr128(((s & 1) ? aO : aE) + (s >> 1) * 64);
    }
  }
  const int li = lane & 15, tq = li >> 2, tp = li & 3, g1 = (lane >> 4) & 1;
  const unsigned wbase = sb + LDS_W + P * W_IMG + (8 * h + tq) * 320 + g1 * 32 + tp * 8;

  bf16x8 qf01[2][2], qf2[2][2];
  bf16x8 kf01[2][2], kf22[2];
  bf16x8 vf[2][4];
  const float qscale = 0.20412414523193154f;
  const f32x16 zero16 = {0, 0, 0, 0, 0, 0, 0, 0, 0, 0, 0, 0, 0, 0, 0, 0};
  {
#pragma unroll
    for (int T = 0; T < 3; ++T) {
      f32x16 a0 = zero16, a1 = zero16;
#pragma unroll
      for (int s = 0; s < 6; ++s) {
        const bf16x8 wf = tr2(wbase + (16 * s) * 320 + T * 64, wbase + (16 * s + 4) * 320 + T * 64);
        a0 = mfma(wf, xf[0][s], a0);
        a1 = mfma(wf, xf[1][s], a1);
      }
      if (T == 0) {
#pragma unroll
        for (int r = 0; r < 16; ++r) { a0[r] *= qscale; a1[r] *= qscale; }
        qf01[0][0] = pack8(a0, 0); qf01[0][1] = pack8(a0, 8);
        qf01[1][0] = pack8(a1, 0); qf01[1][1] = pack8(a1, 8);
      } else if (T == 1) {
        kf01[0][0] = pack8(a0, 0); kf01[0][1] = pack8(a0, 8);
        kf01[1][0] = pack8(a1, 0); kf01[1][1] = pack8(a1, 8);
      } else {
#pragma unroll
        for (int r = 0; r < 8; ++r) { a0[r] *= qscale; a1[r] *= qscale; }
        qf2[0][0] = mk8(cvtpk(a0[0], a0[1]), cvtpk(a0[2], a0[3]), 0u, 0u);
        qf2[0][1] = mk8(0u, 0u, cvtpk(a0[4], a0[5]), cvtpk(a0[6], a0[7]));
        qf2[1][0] = mk8(cvtpk(a1[0], a1[1]), cvtpk(a1[2], a1[3]), 0u, 0u);
        qf2[1][1] = mk8(0u, 0u, cvtpk(a1[4], a1[5]), cvtpk(a1[6], a1[7]));
        kf22[0] = pack8(a0, 8);
        kf22[1] = pack8(a1, 8);
      }
    }
    f32x16 cone;
    {
      const float cv = l31 >= 24 ? 1.0f : 0.0f;
#pragma unroll
      for (int r = 0; r < 16; ++r) cone[r] = cv;
    }
#pragma unroll
    for (int hd = 0; hd < 2; ++hd) {
      f32x16 a0 = cone, a1 = cone;
#pragma unroll
      for (int s = 0; s < 6; ++s) {
        const bf16x8 wf = tr2(wbase + (16 * s) * 320 + (3 + hd) * 64, wbase + (16 * s + 4) * 320 + (3 + hd) * 64);
        a0 = mfma(xf[0][s], wf, a0);
        a1 = mfma(xf[1][s], wf, a1);
      }
      vf[hd][0] = pack8(a0, 0);
      vf[hd][1] = pack8(a0, 8);
      vf[hd][2] = pack8(a1, 0);
      vf[hd][3] = mk8(cvtpk(a1[8], a1[9]), 0u, 0u, 0u);
    }
  }
  STAMP(6);
  asm volatile("s_waitcnt lgkmcnt(0)" ::: "memory");
  __builtin_amdgcn_s_barrier();
  STAMP(7);

  bf16x8 id0, id1;
  {
    const int d0 = l31 - 8 * h;
    const int d1 = l31 - 16 - 8 * h;
    unsigned a[4], c[4];
#pragma unroll
    for (int k = 0; k < 4; ++k) {
      a[k] = (d0 == 2 * k ? 0x3f80u : 0u) | (d0 == 2 * k + 1 ? 0x3f800000u : 0u);
      c[k] = (d1 == 2 * k ? 0x3f80u : 0u) | (d1 == 2 * k + 1 ? 0x3f800000u : 0u);
    }
    id0 = mk8(a[0], a[1], a[2], a[3]);
    id1 = mk8(c[0], c[1], c[2], c[3]);
  }
  const unsigned mdelta = ((unsigned)b * 19208u) & 15u;
  const float LOG2E = 1.4426950408889634f;
  bf16x8 of[2][3];
#pragma unroll
  for (int uu = 0; uu < 2; ++uu) {
    const int u = uu ^ 1 ^ P;
    int q = 32 * u + l31;
    q = q > 48 ? 48 : q;
    f32x16 m0, m1 = zero16;
    {
      const unsigned mb48 = sb + LDS_M + mdelta + wi * 4802 + q * 98;
      const unsigned mb = mb48 + 8 * h;
      float t[25];
      asm volatile(
          "ds_read_u16_d16_hi %0, %25 offset:0\n ds_read_u16_d16_hi %1, %25 offset:2\n ds_read_u16_d16_hi %2, %25 offset:4\n ds_read_u16_d16_hi %3, %25 offset:6\n"
          "ds_read_u16_d16_hi %4, %25 offset:16\n ds_read_u16_d16_hi %5, %25 offset:18\n ds_read_u16_d16_hi %6, %25 offset:20\n ds_read_u16_d16_hi %7, %25 offset:22\n"
          "ds_read_u16_d16_hi %8, %25 offset:32\n ds_read_u16_d16_hi %9, %25 offset:34\n ds_read_u16_d16_hi %10, %25 offset:36\n ds_read_u16_d16_hi %11, %25 offset:38\n"
          "ds_read_u16_d16_hi %12, %25 offset:48\n ds_read_u16_d16_hi %13, %25 offset:50\n ds_read_u16_d16_hi %14, %25 offset:52\n ds_read_u16_d16_hi %15, %25 offset:54\n"
          "ds_read_u16_d16_hi %16, %25 offset:64\n ds_read_u16_d16_hi %17, %25 offset:66\n ds_read_u16_d16_hi %18, %25 offset:68\n ds_read_u16_d16_hi %19, %25 offset:70\n"
          "ds_read_u16_d16_hi %20, %25 offset:80\n ds_read_u16_d16_hi %21, %25 offset:82\n ds_read_u16_d16_hi %22, %25 offset:84\n ds_read_u16_d16_hi %23, %25 offset:86\n"
          "ds_read_u16_d16_hi %24, %26 offset:96\n s_waitcnt lgkmcnt(0)\n"
          : "=&v"(t[0]), "=&v"(t[1]), "=&v"(t[2]), "=&v"(t[3]), "=&v"(t[4]), "=&v"(t[5]), "=&v"(t[6]), "=&v"(t[7]), "=&v"(t[8]), "=&v"(t[9]),
            "=&v"(t[10]), "=&v"(t[11]), "=&v"(t[12]), "=&v"(t[13]), "=&v"(t[14]), "=&v"(t[15]), "=&v"(t[16]), "=&v"(t[17]), "=&v"(t[18]),
            "=&v"(t[19]), "=&v"(t[20]), "=&v"(t[21]), "=&v"(t[22]), "=&v"(t[23]), "=&v"(t[24])
          : "v"(mb), "v"(mb48)
          : "memory");
#pragma unroll
      for (int r = 0; r < 16; ++r) m0[r] = t[r];
#pragma unroll
      for (int r = 0; r < 9; ++r) m1[r] = t[16 + r];
    }
    unsigned og[2][6];
#pragma unroll
    for (int hd = 0; hd < 2; ++hd) {
      const unsigned bb = sb + LDS_B + (2 * P + hd) * 5488 + q * 112;
      const bf16x8 b0 = ldsr128(bb + 16 * h), b1 = ldsr128(bb + 32 + 16 * h), b2 = ldsr128(bb + 64 + 16 * h);
      const float b48 = *LDSP(float, bb + 96 + 4 * h);
      const bf16x8 qa = u ? qf01[1][hd] : qf01[0][hd];
      const bf16x8 qb = u ? qf2[1][hd] : qf2[0][hd];
      f32x16 s0 = mfma(kf01[0][hd], qa, m0);
      f32x16 s1 = mfma(kf01[1][hd], qa, m1);
      s0 = mfma(kf22[0], qb, s0);
      s1 = mfma(kf22[1], qb, s1);
      s0 = mfma(id0, b0, s0);
      s1 = mfma(id0, b2, s1);
      s0 = mfma(id1, b1, s0);
      const float s48 = s1[8] + b48;
      float ma = max3f(s0[0], s0[1], s0[2]), mb_ = max3f(s0[3], s0[4], s0[5]), mc = max3f(s0[6], s0[7], s0[8]);
      ma = max3f(ma, s0[9], s0[10]); mb_ = max3f(mb_, s0[11], s0[12]); mc = max3f(mc, s0[13], s0[14]);
      ma = max3f(ma, s0[15], s1[0]); mb_ = max3f(mb_, s1[1], s1[2]); mc = max3f(mc, s1[3], s1[4]);
      ma = max3f(ma, s1[5], s1[6]); mb_ = max3f(mb_, s1[7], s48);
      float mx = max3f(ma, mb_, mc);
      {
        const unsigned mu = __builtin_bit_cast(unsigned, mx);
        auto sw = __builtin_amdgcn_permlane32_swap(mu, mu, false, false);
        mx = max2f(__builtin_bit_cast(float, sw[0]), __builtin_bit_cast(float, sw[1]));
      }
      const float mneg = -mx * LOG2E;
      float p0[16], p1[9];
#pragma unroll
      for (int r = 0; r < 16; ++r) p0[r] = __builtin_amdgcn_exp2f(__builtin_fmaf(s0[r], LOG2E, mneg));
#pragma unroll
      for (int r = 0; r < 8; ++r) p1[r] = __builtin_amdgcn_exp2f(__builtin_fmaf(s1[r], LOG2E, mneg));
      p1[8] = __builtin_amdgcn_exp2f(__builtin_fmaf(s48, LOG2E, mneg));
      const bf16x8 pf0 = mk8(cvtpk(p0[0], p0[1]), cvtpk(p0[2], p0[3]), cvtpk(p0[4], p0[5]), cvtpk(p0[6], p0[7]));
      const bf16x8 pf1 = mk8(cvtpk(p0[8], p0[9]), cvtpk(p0[10], p0[11]), cvtpk(p0[12], p0[13]), cvtpk(p0[14], p0[15]));
      const bf16x8 pf2 = mk8(cvtpk(p1[0], p1[1]), cvtpk(p1[2], p1[3]), cvtpk(p1[4], p1[5]), cvtpk(p1[6], p1[7]));
      const bf16x8 pf3 = mk8(cvtpk(p1[8], 0.0f), 0u, 0u, 0u);
      f32x16 o = mfma(vf[hd][0], pf0, zero16);
      o = mfma(vf[hd][1], pf1, o);
      o = mfma(vf[hd][2], pf2, o);
      o = mfma(vf[hd][3], pf3, o);
      const float inv = __builtin_amdgcn_rcpf(o[12]);
#pragma unroll
      for (int k = 0; k < 6; ++k) og[hd][k] = cvtpk(o[2 * k] * inv, o[2 * k + 1] * inv);
    }
    const bf16x8 f0 = mk8(og[0][0], og[0][1], og[0][2], og[0][3]);
    const bf16x8 f1 = mk8(og[0][4], og[0][5], og[1][0], og[1][1]);
    const bf16x8 f2 = mk8(og[1][2], og[1][3], og[1][4], og[1][5]);
    if (uu == 0) {
      const unsigned xa = sb + LDS_X + wi * X_WIN + P * 3072 + lane * 16;
      *LDSP(bf16x8, xa) = f0;
      *LDSP(bf16x8, xa + 1024) = f1;
      *LDSP(bf16x8, xa + 2048) = f2;
    } else {
      of[0][0] = f0; of[0][1] = f1; of[0][2] = f2;
    }
  }
  STAMP(8);
  asm volatile("s_waitcnt lgkmcnt(0)" ::: "memory");
  __builtin_amdgcn_s_barrier();
  STAMP(9);
  {
    const unsigned xa = sb + LDS_X + wi * X_WIN + (P ^ 1) * 3072 + lane * 16;
    of[1][0] = ldsr128(xa);
    of[1][1] = ldsr128(xa + 1024);
    of[1][2] = ldsr128(xa + 2048);
  }
  {
    const unsigned wob = sb + LDS_WO + (4 * h + tq) * 192 + (16 * g1 + 8 * (tp & 1) + 4 * (tp >> 1)) * 2;
    f32x16 acc[3] = {zero16, zero16, zero16};
#pragma unroll
    for (int s = 0; s < 6; ++s) {
      const int ss = s % 3;
      const bf16x8 own = of[0][ss], oth = of[1][ss];
      const bool mine = (s / 3) == P;
      const bf16x8 bfr = mine ? own : oth;
#pragma unroll
      for (int T = 0; T < 3; ++T) {
        const bf16x8 wf = tr2(wob + (16 * s) * 192 + 64 * T, wob + (16 * s + 8) * 192 + 64 * T);
        acc[T] = mfma(wf, bfr, acc[T]);
      }
    }
    STAMP(10);
    const int tok = 32 * P + l31;
    if (tok < 49) {
      const int r = tok / 7, cc = tok - 7 * r;
      int gy = wh * 7 + 3 + r;
      gy = gy >= 224 ? gy - 224 : gy;
      int gx = (ww0 + wi) * 7 + 3 + cc;
      gx = gx >= 224 ? gx - 224 : gx;
      char* dst = (char*)out + (gy * 224 + gx) * 192 + 16 * h;
#pragma unroll
      for (int T = 0; T < 3; ++T) {
        *(bf16x8*)(dst + 64 * T) = pack8(acc[T], 0);
        *(bf16x8*)(dst + 64 * T + 32) = pack8(acc[T], 8);
      }
    }
  }
}

extern "C" void kernel_launch(void* const* d_in, const int* in_sizes, int n_in, void* d_out, int out_size, void* d_ws,
                              size_t ws_size, hipStream_t stream) {
  swin_attn_kernel<<<256, 512, 0, stream>>>((const unsigned short*)d_in[0], (const unsigned short*)d_in[1],
                                            (const unsigned short*)d_in[2], (const unsigned short*)d_in[3],
                                            (const int*)d_in[4], (const unsigned short*)d_in[5], (unsigned short*)d_out
  );
}
```

```cpp
#include <hip/hip_runtime.h>
#include <stdint.h>

typedef float f32x16 __attribute__((ext_vector_type(16)));
typedef float f32x2 __attribute__((ext_vector_type(2)));
typedef __bf16 bf16x8 __attribute__((ext_vector_type(8)));
typedef __bf16 bf16x2 __attribute__((ext_vector_type(2)));
typedef short s16x4 __attribute__((ext_vector_type(4)));
typedef unsigned u32x4 __attribute__((ext_vector_type(4)));
typedef unsigned u32x2 __attribute__((ext_vector_type(2)));

#if defined(__HIP_DEVICE_COMPILE__)
#define NOPK __attribute__((target("no-packed-fp32-ops")))
#else
#define NOPK
#endif
#define DI __device__ __forceinline__ NOPK
#define LDSP(T, a) ((__attribute__((address_space(3))) T*)(a))

constexpr int LDS_W = 0;
constexpr int W_IMG = 96 * 320;
constexpr int LDS_WO = 2 * W_IMG;
constexpr int LDS_X = LDS_WO + 18432;
constexpr int X_WIN = 49 * 192;
constexpr int LDS_M = LDS_X + 4 * X_WIN;
constexpr int LDS_B = LDS_M + 1224 * 16;
constexpr int LDS_RPB = LDS_B + 4 * 49 * 112;
constexpr int LDS_FLAG = LDS_RPB + 1360;
constexpr int LDS_LP = LDS_FLAG + 128;
constexpr int LDS_TOTAL = LDS_LP + 2560;

#define WAIT_VM(n) __builtin_amdgcn_s_waitcnt(0x0F70 | ((n) & 15) | (((n) >> 4) << 14))
#define WAIT_LGKM0() __builtin_amdgcn_s_waitcnt(0xC07F)
#define FENCE() asm volatile("" ::: "memory")

DI unsigned cvtpk(float lo, float hi) {
  f32x2 v = {lo, hi};
  bf16x2 b = __builtin_convertvector(v, bf16x2);
  return __builtin_bit_cast(unsigned, b);
}
DI float max3f(float a, float b, float c) {
  float r;
  asm("v_max3_f32 %0, %1, %2, %3" : "=v"(r) : "v"(a), "v"(b), "v"(c));
  return r;
}
DI float max2f(float a, float b) {
  float r;
  asm("v_max_f32_e32 %0, %1, %2" : "=v"(r) : "v"(a), "v"(b));
  return r;
}
DI bf16x8 mk8(unsigned a, unsigned b, unsigned c, unsigned d) {
  u32x4 v = {a, b, c, d};
  return __builtin_bit_cast(bf16x8, v);
}
DI bf16x8 pack8(const f32x16& a, int r0) {
  return mk8(cvtpk(a[r0], a[r0 + 1]), cvtpk(a[r0 + 2], a[r0 + 3]), cvtpk(a[r0 + 4], a[r0 + 5]), cvtpk(a[r0 + 6], a[r0 + 7]));
}
DI f32x16 mfma(bf16x8 a, bf16x8 b, f32x16 c) { return __builtin_amdgcn_mfma_f32_32x32x16_bf16(a, b, c, 0, 0, 0); }

DI bf16x8 tr2(unsigned addr0, unsigned addr1) {
  s16x4 lo = __builtin_amdgcn_ds_read_tr16_b64_v4i16(LDSP(s16x4, addr0));
  s16x4 hi = __builtin_amdgcn_ds_read_tr16_b64_v4i16(LDSP(s16x4, addr1));
  u32x2 a = __builtin_bit_cast(u32x2, lo), b = __builtin_bit_cast(u32x2, hi);
  return mk8(a[0], a[1], b[0], b[1]);
}
DI bf16x8 ldsr128(unsigned addr) { return *LDSP(bf16x8, addr); }

#define STAMP(k)
#define SB() __builtin_amdgcn_sched_barrier(0)

DI void dma16(const void* g, unsigned ldsaddr) {
  __builtin_amdgcn_global_load_lds((const __attribute__((address_space(1))) void*)g, LDSP(void, ldsaddr), 16, 0, 0);
}
DI void flag_set(unsigned addr) {
  FENCE();
  *LDSP(volatile unsigned, addr) = 1u;
  FENCE();
}
DI void flag_wait(unsigned addr) {
  FENCE();
  while (__builtin_amdgcn_readfirstlane(*LDSP(volatile unsigned, addr)) == 0u) __builtin_amdgcn_s_sleep(1);
  FENCE();
}

struct MaskRegs { float t[25]; };
DI void mask_issue(MaskRegs& m, unsigned mb, unsigned mb48) {
  float* t = m.t;
  asm volatile(
      "ds_read_u16_d16_hi %0, %25 offset:0\n ds_read_u16_d16_hi %1, %25 offset:2\n ds_read_u16_d16_hi %2, %25 offset:4\n ds_read_u16_d16_hi %3, %25 offset:6\n"
      "ds_read_u16_d16_hi %4, %25 offset:16\n ds_read_u16_d16_hi %5, %25 offset:18\n ds_read_u16_d16_hi %6, %25 offset:20\n ds_read_u16_d16_hi %7, %25 offset:22\n"
      "ds_read_u16_d16_hi %8, %25 offset:32\n ds_read_u16_d16_hi %9, %25 offset:34\n ds_read_u16_d16_hi %10, %25 offset:36\n ds_read_u16_d16_hi %11, %25 offset:38\n"
      "ds_read_u16_d16_hi %12, %25 offset:48\n ds_read_u16_d16_hi %13, %25 offset:50\n ds_read_u16_d16_hi %14, %25 offset:52\n ds_read_u16_d16_hi %15, %25 offset:54\n"
      "ds_read_u16_d16_hi %16, %25 offset:64\n ds_read_u16_d16_hi %17, %25 offset:66\n ds_read_u16_d16_hi %18, %25 offset:68\n ds_read_u16_d16_hi %19, %25 offset:70\n"
      "ds_read_u16_d16_hi %20, %25 offset:80\n ds_read_u16_d16_hi %21, %25 offset:82\n ds_read_u16_d16_hi %22, %25 offset:84\n ds_read_u16_d16_hi %23, %25 offset:86\n"
      "ds_read_u16_d16_hi %24, %26 offset:96\n"
      : "=&v"(t[0]), "=&v"(t[1]), "=&v"(t[2]), "=&v"(t[3]), "=&v"(t[4]), "=&v"(t[5]), "=&v"(t[6]), "=&v"(t[7]), "=&v"(t[8]), "=&v"(t[9]),
        "=&v"(t[10]), "=&v"(t[11]), "=&v"(t[12]), "=&v"(t[13]), "=&v"(t[14]), "=&v"(t[15]), "=&v"(t[16]), "=&v"(t[17]), "=&v"(t[18]),
        "=&v"(t[19]), "=&v"(t[20]), "=&v"(t[21]), "=&v"(t[22]), "=&v"(t[23]), "=&v"(t[24])
      : "v"(mb), "v"(mb48)
      : "memory");
}
DI void mask_wait(MaskRegs& m, f32x16& m0, f32x16& m1) {
  float* t = m.t;
  asm volatile("s_waitcnt lgkmcnt(0)"
               : "+v"(t[0]), "+v"(t[1]), "+v"(t[2]), "+v"(t[3]), "+v"(t[4]), "+v"(t[5]), "+v"(t[6]), "+v"(t[7]), "+v"(t[8]), "+v"(t[9]),
                 "+v"(t[10]), "+v"(t[11]), "+v"(t[12]), "+v"(t[13]), "+v"(t[14]), "+v"(t[15]), "+v"(t[16]), "+v"(t[17]), "+v"(t[18]),
                 "+v"(t[19]), "+v"(t[20]), "+v"(t[21]), "+v"(t[22]), "+v"(t[23]), "+v"(t[24])
               :
               : "memory");
#pragma unroll
  for (int r = 0; r < 16; ++r) m0[r] = t[r];
#pragma unroll
  for (int r = 0; r < 9; ++r) m1[r] = t[16 + r];
#pragma unroll
  for (int r = 9; r < 16; ++r) m1[r] = t[16];
}

struct Bias { bf16x8 b0, b1, b2; float b48; };
DI Bias bias_load(unsigned bb, int h) {
  Bias r;
  r.b0 = ldsr128(bb + 16 * h); r.b1 = ldsr128(bb + 32 + 16 * h); r.b2 = ldsr128(bb + 64 + 16 * h);
  r.b48 = *LDSP(float, bb + 96 + 4 * h);
  return r;
}
struct SU { f32x16 s0, s1; float b48; };
struct PU { bf16x8 p0, p1, p2, p3; };

DI SU s_unit(bf16x8 k0, bf16x8 k1, bf16x8 k20, bf16x8 k21, bf16x8 qa, bf16x8 qb, const f32x16& m0, const f32x16& m1,
             bf16x8 id0, bf16x8 id1, const Bias& b) {
  SU u;
  u.s0 = mfma(k0, qa, m0);
  u.s1 = mfma(k1, qa, m1);
  u.s0 = mfma(k20, qb, u.s0);
  u.s1 = mfma(k21, qb, u.s1);
  u.s0 = mfma(id0, b.b0, u.s0);
  u.s1 = mfma(id0, b.b2, u.s1);
  u.s0 = mfma(id1, b.b1, u.s0);
  u.b48 = b.b48;
  return u;
}
DI PU softmax_unit(const SU& u) {
  const float LOG2E = 1.4426950408889634f;
  const f32x16& s0 = u.s0; const f32x16& s1 = u.s1;
  const float s48 = s1[8] + u.b48;
  float ma = max3f(s0[0], s0[1], s0[2]), mb = max3f(s0[3], s0[4], s0[5]), mc = max3f(s0[6], s0[7], s0[8]);
  ma = max3f(ma, s0[9], s0[10]); mb = max3f(mb, s0[11], s0[12]); mc = max3f(mc, s0[13], s0[14]);
  ma = max3f(ma, s0[15], s1[0]); mb = max3f(mb, s1[1], s1[2]); mc = max3f(mc, s1[3], s1[4]);
  ma = max3f(ma, s1[5], s1[6]); mb = max3f(mb, s1[7], s48);
  float mx = max3f(ma, mb, mc);
  {
    const unsigned mu = __float_as_uint(mx);
    auto sw = __builtin_amdgcn_permlane32_swap(mu, mu, false, false);
    const unsigned w0 = sw[0], w1 = sw[1];
    mx = max2f(__uint_as_float(w0), __uint_as_float(w1));
  }
  const float mneg = -mx * LOG2E;
  float p0[16], p1[9];
#pragma unroll
  for (int r = 0; r < 16; ++r) p0[r] = __builtin_amdgcn_exp2f(__builtin_fmaf(s0[r], LOG2E, mneg));
#pragma unroll
  for (int r = 0; r < 8; ++r) p1[r] = __builtin_amdgcn_exp2f(__builtin_fmaf(s1[r], LOG2E, mneg));
  p1[8] = __builtin_amdgcn_exp2f(__builtin_fmaf(s48, LOG2E, mneg));
  PU p;
  p.p0 = mk8(cvtpk(p0[0], p0[1]), cvtpk(p0[2], p0[3]), cvtpk(p0[4], p0[5]), cvtpk(p0[6], p0[7]));
  p.p1 = mk8(cvtpk(p0[8], p0[9]), cvtpk(p0[10], p0[11]), cvtpk(p0[12], p0[13]), cvtpk(p0[14], p0[15]));
  p.p2 = mk8(cvtpk(p1[0], p1[1]), cvtpk(p1[2], p1[3]), cvtpk(p1[4], p1[5]), cvtpk(p1[6], p1[7]));
  p.p3 = mk8(cvtpk(p1[8], 0.0f), 0u, 0u, 0u);
  return p;
}
DI f32x16 pv_unit(const bf16x8* v, const PU& p) {
  const f32x16 zero16 = {0, 0, 0, 0, 0, 0, 0, 0, 0, 0, 0, 0, 0, 0, 0, 0};
  f32x16 o = mfma(v[0], p.p0, zero16);
  o = mfma(v[1], p.p1, o);
  o = mfma(v[2], p.p2, o);
  o = mfma(v[3], p.p3, o);
  return o;
}
DI void norm_unit(const f32x16& o, unsigned* og) {
  const float inv = __builtin_amdgcn_rcpf(o[12]);
#pragma unroll
  for (int k = 0; k < 6; ++k) og[k] = cvtpk(o[2 * k] * inv, o[2 * k + 1] * inv);
}

__global__ __launch_bounds__(512, 2) NOPK void swin_attn_kernel(
    const unsigned short* __restrict__ x, const unsigned short* __restrict__ wqkv, const unsigned short* __restrict__ wo,
    const unsigned short* __restrict__ rpb, const int* __restrict__ rp_idx, const unsigned short* __restrict__ mask,
    unsigned short* __restrict__ out
    ) {
  __shared__ __attribute__((aligned(16))) char smem[LDS_TOTAL];
  const unsigned sb = (unsigned)(uintptr_t)LDSP(char, smem);
  const int tid = threadIdx.x;
  const int lane = tid & 63;
  const int wv = __builtin_amdgcn_readfirstlane(tid >> 6);
  const int b = blockIdx.x;
  const int wh = b >> 3, ww0 = (b & 7) * 4;
  const int h = lane >> 5, l31 = lane & 31;
  const int wi = wv >> 1, P = wv & 1;
  const bool late = wv >= 4;
  const unsigned mdelta = ((unsigned)b * 19208u) & 15u;
  const unsigned moff = ((unsigned)b * 19208u) & ~15u;

  const int ww = ww0 + wi;
  const unsigned base0 = (unsigned)(((wh * 7 + 3) * 224 + ww * 7 + 3) * 192);
  const unsigned wrapY = wh == 31 ? 9633792u : 0u, wrapX = ww == 31 ? 43008u : 0u;
  auto fix = [&](unsigned lp) NOPK -> unsigned {
    unsigned o = base0 + (lp & ~15u);
    if (wrapY | wrapX) o -= ((lp & 1u) ? wrapY : 0u) + ((lp & 2u) ? wrapX : 0u);
    return o;
  };
  unsigned voff[6];
#pragma unroll
  for (int jj = 0; jj < 6; ++jj) {
    const unsigned i = 64u * (6 * P + jj) + lane;
    const unsigned g = ((i * 1366u) >> 16) & 3u;
    const unsigned r = (i * 781u) >> 16;
    const unsigned tok = (i * 5462u) >> 16;
    const unsigned lp = r * 41664u + ((i ^ g) << 4) + (r >= 4 ? 1u : 0u) + (tok - 7 * r >= 4 ? 2u : 0u);
    if (wi == 0 && (jj < 4 || P == 0)) *LDSP(unsigned, sb + LDS_LP + (6 * P + jj) * 256 + lane * 4) = lp;
    voff[jj] = fix(lp);
  }
  const unsigned xreg = sb + LDS_X + wi * X_WIN;
  auto issue_x = [&]() NOPK {
#pragma unroll
    for (int jj = 0; jj < 6; ++jj) {
      if (jj < 3) dma16((const char*)x + voff[jj], xreg + (6 * P + jj) * 1024);
      else if (jj == 3) { if (P == 0 || lane < 12) dma16((const char*)x + voff[jj], xreg + (6 * P + jj) * 1024); }
      else if (P == 0) dma16((const char*)x + voff[jj], xreg + jj * 1024);
    }
  };
  auto issue_m = [&]() NOPK {
    const unsigned cs = (mdelta + 4802u * wi) >> 4;
#pragma unroll
    for (int rd = 0; rd < 3; ++rd) {
      if (rd < 2 || P == 0) {
        const unsigned c = cs + 64u * (P + 2 * rd);
        unsigned gc = (moff >> 4) + c + lane;
        gc = gc > 307327u ? 307327u : gc;
        dma16((const char*)mask + (size_t)gc * 16, sb + LDS_M + c * 16);
      }
    }
  };
  const int r3 = lane / 20, ch = lane - r3 * 20;
  unsigned wsrc;
  {
    const unsigned long long T0 = (0ull << 0) | (1ull << 5) | (3ull << 10) | (4ull << 15) | (12ull << 20) | (13ull << 25) | (15ull << 30) |
                                  (16ull << 35) | (2ull << 40) | (5ull << 45) | (14ull << 50) | (17ull << 55);
    const unsigned long long T1 = (24ull << 0) | (25ull << 5) | (26ull << 10) | (0ull << 15) | (27ull << 20) | (28ull << 25) | (29ull << 30);
    const unsigned gam = (unsigned)((ch < 12 ? (T0 >> (5 * ch)) : (T1 >> (5 * (ch - 12)))) & 31ull);
    wsrc = r3 * 576 + gam * 16;
  }
  const bool wact = lane < 60 && ch != 15 && ch != 19;

  int ia[5], ib[5], i48 = 0;
  if (!late) {
    issue_x();
    if (wact) {
#pragma unroll
      for (int j = 0; j < 4; ++j) {
        const int pc = 48 + 4 * wv + j;
        dma16((const char*)wqkv + wsrc + (pc & 31) * 1728 + (pc >> 5) * 96, sb + LDS_W + pc * 960);
      }
    }
    issue_m();
#pragma unroll
    for (int j = 0; j < 5; ++j) {
      if (j < 4 || P == 1) {
        const int pc = (wi * 9 + P * 4) + j;
        dma16((const char*)wo + pc * 1024 + lane * 16, sb + LDS_WO + pc * 1024);
      }
    }
    if (tid < 32) *LDSP(unsigned, sb + LDS_FLAG + tid * 4) = 0u;
    STAMP(2);
    WAIT_VM(7);
    WAIT_LGKM0();
  } else {
    const int t = tid - 256;
    u32x2 rv = {0, 0};
    if (t < 169) rv = *(const u32x2*)(rpb + t * 4);
#pragma unroll
    for (int rd = 0; rd < 5; ++rd) {
      const int e = t + 256 * rd;
      const int q = (e * 2731) >> 16, kp = e - 24 * q;
      ia[rd] = 0; ib[rd] = 0;
      if (e < 1176) { ia[rd] = rp_idx[q * 49 + 2 * kp]; ib[rd] = rp_idx[q * 49 + 2 * kp + 1]; }
    }
    if (t < 49) i48 = rp_idx[t * 49 + 48];
    if (wact) {
#pragma unroll
      for (int j = 0; j < 12; ++j) {
        const int pc = 12 * (wv - 4) + j;
        dma16((const char*)wqkv + wsrc + (pc & 31) * 1728 + (pc >> 5) * 96, sb + LDS_W + pc * 960);
      }
    }
#pragma unroll
    for (int rd = 0; rd < 2; ++rd) {
      const int z = t + 256 * rd;
      if (z < 384) *LDSP(u32x4, sb + LDS_W + (z >> 1) * 320 + (15 + 4 * (z & 1)) * 16) = u32x4{0, 0, 0, 0};
    }
    WAIT_VM(0);
#pragma unroll
    for (int rd = 0; rd < 5; ++rd) asm volatile("" : "+v"(ia[rd]), "+v"(ib[rd]));
    asm volatile("" : "+v"(i48));
    if (t < 169) *LDSP(u32x2, sb + LDS_RPB + t * 8) = rv;
    issue_x();
    issue_m();
    STAMP(2);
    WAIT_LGKM0();
  }
  FENCE();
  __builtin_amdgcn_s_barrier();
  FENCE();
  STAMP(3);
  if (late) {
    const int t = tid - 256;
#pragma unroll
    for (int rd = 0; rd < 5; ++rd) {
      const int e = t + 256 * rd;
      const int q = (e * 2731) >> 16, kp = e - 24 * q;
      if (e < 1176) {
        const u32x2 va = *LDSP(u32x2, sb + LDS_RPB + ia[rd] * 8);
        const u32x2 vb = *LDSP(u32x2, sb + LDS_RPB + ib[rd] * 8);
        const unsigned dst = sb + LDS_B + q * 112 + kp * 4;
        *LDSP(unsigned, dst + 0 * 5488) = __builtin_amdgcn_perm(vb[0], va[0], 0x05040100u);
        *LDSP(unsigned, dst + 1 * 5488) = __builtin_amdgcn_perm(vb[0], va[0], 0x07060302u);
        *LDSP(unsigned, dst + 2 * 5488) = __builtin_amdgcn_perm(vb[1], va[1], 0x05040100u);
        *LDSP(unsigned, dst + 3 * 5488) = __builtin_amdgcn_perm(vb[1], va[1], 0x07060302u);
      }
    }
    if (t < 49) {
      const u32x2 va = *LDSP(u32x2, sb + LDS_RPB + i48 * 8);
      const unsigned dst = sb + LDS_B + t * 112 + 96;
      *LDSP(u32x2, dst + 0 * 5488) = u32x2{va[0] << 16, 0xff800000u};
      *LDSP(u32x2, dst + 1 * 5488) = u32x2{va[0] & 0xffff0000u, 0xff800000u};
      *LDSP(u32x2, dst + 2 * 5488) = u32x2{va[1] << 16, 0xff800000u};
      *LDSP(u32x2, dst + 3 * 5488) = u32x2{va[1] & 0xffff0000u, 0xff800000u};
    }
    WAIT_LGKM0();
    FENCE();
    __builtin_amdgcn_s_barrier();
    FENCE();
    if (P == 0) WAIT_VM(3); else WAIT_VM(2);
    flag_set(sb + LDS_FLAG + (wv * 4 + 0) * 4);
    flag_wait(sb + LDS_FLAG + ((wv ^ 1) * 4 + 0) * 4);
  }
  STAMP(4);

  bf16x8 xf[2][6];
  {
#pragma unroll
    for (int u = 0; u < 2; ++u) {
      int tok = 32 * u + l31;
      tok = tok > 48 ? 48 : tok;
      const unsigned base = xreg + tok * 192;
      const unsigned sw = (unsigned)(h ^ ((tok >> 2) & 3));
      const unsigned aE = base + sw * 16, aO = base + (sw ^ 2) * 16;
#pragma unroll
      for (int s = 0; s < 6; ++s) xf[u][s] = ldsr128(((s & 1) ? aO : aE) + (s >> 1) * 64);
    }
  }
  const int li = lane & 15, tq = li >> 2, tp = li & 3, g1 = (lane >> 4) & 1;
  const unsigned wbase = sb + LDS_W + P * W_IMG + (8 * h + tq) * 320 + g1 * 32 + tp * 8;

  bf16x8 qf01[2][2], qf2[2][2];
  bf16x8 kf01[2][2], kf22[2];
  bf16x8 vf[2][4];
  const float qscale = 0.20412414523193154f;
  const f32x16 zero16 = {0, 0, 0, 0, 0, 0, 0, 0, 0, 0, 0, 0, 0, 0, 0, 0};
  {
#pragma unroll
    for (int T = 0; T < 3; ++T) {
      f32x16 a0 = zero16, a1 = zero16;
#pragma unroll
      for (int s = 0; s < 6; ++s) {
        const bf16x8 wf = tr2(wbase + (16 * s) * 320 + T * 64, wbase + (16 * s + 4) * 320 + T * 64);
        a0 = mfma(wf, xf[0][s], a0);
        a1 = mfma(wf, xf[1][s], a1);
      }
      if (T == 0) {
#pragma unroll
        for (int r = 0; r < 16; ++r) { a0[r] *= qscale; a1[r] *= qscale; }
        qf01[0][0] = pack8(a0, 0); qf01[0][1] = pack8(a0, 8);
        qf01[1][0] = pack8(a1, 0); qf01[1][1] = pack8(a1, 8);
      } else if (T == 1) {
        kf01[0][0] = pack8(a0, 0); kf01[0][1] = pack8(a0, 8);
        kf01[1][0] = pack8(a1, 0); kf01[1][1] = pack8(a1, 8);
      } else {
#pragma unroll
        for (int r = 0; r < 8; ++r) { a0[r] *= qscale; a1[r] *= qscale; }
        qf2[0][0] = mk8(cvtpk(a0[0], a0[1]), cvtpk(a0[2], a0[3]), 0u, 0u);
        qf2[0][1] = mk8(0u, 0u, cvtpk(a0[4], a0[5]), cvtpk(a0[6], a0[7]));
        qf2[1][0] = mk8(cvtpk(a1[0], a1[1]), cvtpk(a1[2], a1[3]), 0u, 0u);
        qf2[1][1] = mk8(0u, 0u, cvtpk(a1[4], a1[5]), cvtpk(a1[6], a1[7]));
        kf22[0] = pack8(a0, 8);
        kf22[1] = pack8(a1, 8);
      }
    }
    f32x16 cone;
    {
      const float cv = l31 >= 24 ? 1.0f : 0.0f;
#pragma unroll
      for (int r = 0; r < 16; ++r) cone[r] = cv;
    }
#pragma unroll
    for (int hd = 0; hd < 2; ++hd) {
      f32x16 a0 = cone, a1 = cone;
#pragma unroll
      for (int s = 0; s < 6; ++s) {
        const bf16x8 wf = tr2(wbase + (16 * s) * 320 + (3 + hd) * 64, wbase + (16 * s + 4) * 320 + (3 + hd) * 64);
        a0 = mfma(xf[0][s], wf, a0);
        a1 = mfma(xf[1][s], wf, a1);
      }
      vf[hd][0] = pack8(a0, 0);
      vf[hd][1] = pack8(a0, 8);
      vf[hd][2] = pack8(a1, 0);
      vf[hd][3] = mk8(cvtpk(a1[8], a1[9]), 0u, 0u, 0u);
    }
  }
  STAMP(5);
  WAIT_VM(0);
  if (late) {
    flag_set(sb + LDS_FLAG + (wv * 4 + 1) * 4);
    flag_wait(sb + LDS_FLAG + ((wv ^ 1) * 4 + 1) * 4);
  } else {
    FENCE();
    __builtin_amdgcn_s_barrier();
    FENCE();
  }
  STAMP(6);

  bf16x8 id0, id1;
  {
    const int d0 = l31 - 8 * h, d1 = l31 - 16 - 8 * h;
    unsigned a[4], c[4];
#pragma unroll
    for (int k = 0; k < 4; ++k) {
      a[k] = (d0 == 2 * k ? 0x3f80u : 0u) | (d0 == 2 * k + 1 ? 0x3f800000u : 0u);
      c[k] = (d1 == 2 * k ? 0x3f80u : 0u) | (d1 == 2 * k + 1 ? 0x3f800000u : 0u);
    }
    id0 = mk8(a[0], a[1], a[2], a[3]);
    id1 = mk8(c[0], c[1], c[2], c[3]);
  }
  const int q1 = 32 + l31 > 48 ? 48 : 32 + l31;
  const unsigned mbase = sb + LDS_M + mdelta + wi * 4802;
  const unsigned bbase = sb + LDS_B + (2 * P) * 5488;
  const unsigned exb = xreg + lane * 16;

  MaskRegs mr;
  f32x16 m0, m1;
  unsigned ogA[6], ogB[6];
  mask_issue(mr, mbase + l31 * 98 + 8 * h, mbase + l31 * 98);
  Bias bi0 = bias_load(bbase + l31 * 112, h);
  Bias bi1 = bias_load(bbase + 5488 + l31 * 112, h);
  mask_wait(mr, m0, m1);
  SB();
  SU su0 = s_unit(kf01[0][0], kf01[1][0], kf22[0], kf22[1], qf01[0][0], qf2[0][0], m0, m1, id0, id1, bi0);
  SB();
  SU su1 = s_unit(kf01[0][1], kf01[1][1], kf22[0], kf22[1], qf01[0][1], qf2[0][1], m0, m1, id0, id1, bi1);
  PU pu0 = softmax_unit(su0);
  Bias bi2 = bias_load(bbase + q1 * 112, h);
  SB();
  mask_issue(mr, mbase + q1 * 98 + 8 * h, mbase + q1 * 98);
  f32x16 o0 = pv_unit(vf[0], pu0);
  Bias bi3 = bias_load(bbase + 5488 + q1 * 112, h);
  mask_wait(mr, m0, m1);
  SB();
  SU su2 = s_unit(kf01[0][0], kf01[1][0], kf22[0], kf22[1], qf01[1][0], qf2[1][0], m0, m1, id0, id1, bi2);
  PU pu1 = softmax_unit(su1);
  norm_unit(o0, ogA);
  SB();
  f32x16 o1 = pv_unit(vf[1], pu1);
  SU su3 = s_unit(kf01[0][1], kf01[1][1], kf22[0], kf22[1], qf01[1][1], qf2[1][1], m0, m1, id0, id1, bi3);
  PU pu2 = softmax_unit(su2);
  norm_unit(o1, ogB);
  *LDSP(bf16x8, exb + P * 3072) = mk8(ogA[0], ogA[1], ogA[2], ogA[3]);
  *LDSP(bf16x8, exb + P * 3072 + 1024) = mk8(ogA[4], ogA[5], ogB[0], ogB[1]);
  *LDSP(bf16x8, exb + P * 3072 + 2048) = mk8(ogB[2], ogB[3], ogB[4], ogB[5]);
  SB();
  WAIT_LGKM0();
  flag_set(sb + LDS_FLAG + (wv * 4 + 2) * 4);
  SB();
  f32x16 o2 = pv_unit(vf[0], pu2);
  PU pu3 = softmax_unit(su3);
  norm_unit(o2, ogA);
  SB();
  f32x16 o3 = pv_unit(vf[1], pu3);
  norm_unit(o3, ogB);
  const bf16x8 t1f0 = mk8(ogA[0], ogA[1], ogA[2], ogA[3]);
  const bf16x8 t1f1 = mk8(ogA[4], ogA[5], ogB[0], ogB[1]);
  const bf16x8 t1f2 = mk8(ogB[2], ogB[3], ogB[4], ogB[5]);
  STAMP(7);

  const unsigned wob = sb + LDS_WO + (4 * h + tq) * 192 + (16 * g1 + 8 * (tp & 1) + 4 * (tp >> 1)) * 2;
  f32x16 acc[3] = {zero16, zero16, zero16};
  if (P == 0) {
    *LDSP(bf16x8, exb + 6144) = t1f0;
    *LDSP(bf16x8, exb + 6144 + 1024) = t1f1;
    *LDSP(bf16x8, exb + 6144 + 2048) = t1f2;
    WAIT_LGKM0();
    flag_set(sb + LDS_FLAG + (wv * 4 + 3) * 4);
    flag_wait(sb + LDS_FLAG + ((wv ^ 1) * 4 + 2) * 4);
#pragma unroll
    for (int s = 0; s < 6; ++s) {
      const bf16x8 bfr = ldsr128(exb + s * 1024);
#pragma unroll
      for (int T = 0; T < 3; ++T)
        acc[T] = mfma(tr2(wob + (16 * s) * 192 + 64 * T, wob + (16 * s + 8) * 192 + 64 * T), bfr, acc[T]);
    }
  } else {
#pragma unroll
    for (int s = 3; s < 6; ++s) {
      const bf16x8 bfr = s == 3 ? t1f0 : (s == 4 ? t1f1 : t1f2);
#pragma unroll
      for (int T = 0; T < 3; ++T)
        acc[T] = mfma(tr2(wob + (16 * s) * 192 + 64 * T, wob + (16 * s + 8) * 192 + 64 * T), bfr, acc[T]);
    }
    flag_wait(sb + LDS_FLAG + ((wv ^ 1) * 4 + 3) * 4);
#pragma unroll
    for (int s = 0; s < 3; ++s) {
      const bf16x8 bfr = ldsr128(exb + 6144 + s * 1024);
#pragma unroll
      for (int T = 0; T < 3; ++T)
        acc[T] = mfma(tr2(wob + (16 * s) * 192 + 64 * T, wob + (16 * s + 8) * 192 + 64 * T), bfr, acc[T]);
    }
  }
  STAMP(8);
  {
    int tok = 32 * P + l31;
    const bool tv = tok < 49;
    tok = tv ? tok : 48;
    const unsigned g = (unsigned)((tok >> 2) & 3);
    const unsigned base = xreg + tok * 192;
    const unsigned a0 = base + ((h ^ g) << 4), a1 = base + (((2 + h) ^ g) << 4);
    if (tv) {
#pragma unroll
      for (int T = 0; T < 3; ++T) {
        *LDSP(bf16x8, a0 + 64 * T) = pack8(acc[T], 0);
        *LDSP(bf16x8, a1 + 64 * T) = pack8(acc[T], 8);
      }
    }
    const unsigned rb = xreg + P * 6144 + lane * 16;
    const unsigned lpb = sb + LDS_LP + P * 1536 + lane * 4;
#pragma unroll
    for (int jj = 0; jj < 6; ++jj) {
      if (jj < 3 || P == 0 || (jj == 3 && lane < 12)) {
        const unsigned o = fix(*LDSP(unsigned, lpb + jj * 256));
        { const bf16x8 vv = ldsr128(rb + jj * 1024); const char* pp = (const char*)out + o;
          asm volatile("global_store_dwordx4 %0, %1, off sc1" :: "v"(pp), "v"(vv) : "memory"); }
      }
    }
  }
}

extern "C" void kernel_launch(void* const* d_in, const int* in_sizes, int n_in, void* d_out, int out_size, void* d_ws,
                              size_t ws_size, hipStream_t stream) {
  swin_attn_kernel<<<256, 512, 0, stream>>>((const unsigned short*)d_in[0], (const unsigned short*)d_in[1],
                                            (const unsigned short*)d_in[2], (const unsigned short*)d_in[3],
                                            (const int*)d_in[4], (const unsigned short*)d_in[5], (unsigned short*)d_out
  );
}
```

```cpp
#include <hip/hip_runtime.h>
#include <stdint.h>

typedef float f32x16 __attribute__((ext_vector_type(16)));
typedef float f32x2 __attribute__((ext_vector_type(2)));
typedef __bf16 bf16x8 __attribute__((ext_vector_type(8)));
typedef __bf16 bf16x2 __attribute__((ext_vector_type(2)));
typedef short s16x4 __attribute__((ext_vector_type(4)));
typedef unsigned u32x4 __attribute__((ext_vector_type(4)));
typedef unsigned u32x2 __attribute__((ext_vector_type(2)));

#if defined(__HIP_DEVICE_COMPILE__)
#define NOPK __attribute__((target("no-packed-fp32-ops")))
#else
#define NOPK
#endif
#define DI __device__ __forceinline__ NOPK
#define LDSP(T, a) ((__attribute__((address_space(3))) T*)(a))

constexpr int LDS_W = 0;
constexpr int W_IMG = 5 * 6144;
constexpr int LDS_WO = 2 * W_IMG;
constexpr int LDS_X = LDS_WO + 18432;
constexpr int X_WIN = 49 * 192;
constexpr int LDS_M = LDS_X + 4 * X_WIN;
constexpr int LDS_B = LDS_M + 1224 * 16;
constexpr int LDS_RPB = LDS_B + 4 * 49 * 112;
constexpr int LDS_FLAG = LDS_RPB + 1360;
constexpr int LDS_LP = LDS_FLAG + 128;
constexpr int LDS_TOTAL = LDS_LP + 2560;

#define WAIT_VM(n) __builtin_amdgcn_s_waitcnt(0x0F70 | ((n) & 15) | (((n) >> 4) << 14))
#define WAIT_LGKM0() __builtin_amdgcn_s_waitcnt(0xC07F)
#define FENCE() asm volatile("" ::: "memory")

DI unsigned cvtpk(float lo, float hi) {
  f32x2 v = {lo, hi};
  bf16x2 b = __builtin_convertvector(v, bf16x2);
  return __builtin_bit_cast(unsigned, b);
}
DI float max3f(float a, float b, float c) {
  float r;
  asm("v_max3_f32 %0, %1, %2, %3" : "=v"(r) : "v"(a), "v"(b), "v"(c));
  return r;
}
DI float max2f(float a, float b) {
  float r;
  asm("v_max_f32_e32 %0, %1, %2" : "=v"(r) : "v"(a), "v"(b));
  return r;
}
DI bf16x8 mk8(unsigned a, unsigned b, unsigned c, unsigned d) {
  u32x4 v = {a, b, c, d};
  return __builtin_bit_cast(bf16x8, v);
}
DI bf16x8 pack8(const f32x16& a, int r0) {
  return mk8(cvtpk(a[r0], a[r0 + 1]), cvtpk(a[r0 + 2], a[r0 + 3]), cvtpk(a[r0 + 4], a[r0 + 5]), cvtpk(a[r0 + 6], a[r0 + 7]));
}
DI f32x16 mfma(bf16x8 a, bf16x8 b, f32x16 c) { return __builtin_amdgcn_mfma_f32_32x32x16_bf16(a, b, c, 0, 0, 0); }

DI bf16x8 tr2(unsigned addr0, unsigned addr1) {
  s16x4 lo = __builtin_amdgcn_ds_read_tr16_b64_v4i16(LDSP(s16x4, addr0));
  s16x4 hi = __builtin_amdgcn_ds_read_tr16_b64_v4i16(LDSP(s16x4, addr1));
  u32x2 a = __builtin_bit_cast(u32x2, lo), b = __builtin_bit_cast(u32x2, hi);
  return mk8(a[0], a[1], b[0], b[1]);
}
DI bf16x8 ldsr128(unsigned addr) { return *LDSP(bf16x8, addr); }

#define STAMP(k)
#define SB() __builtin_amdgcn_sched_barrier(0)

DI void dma16(const void* sbase, unsigned voff, unsigned lds) {
  asm volatile("s_mov_b32 m0, %2\n\ts_nop 0\n\tglobal_load_lds_dwordx4 %0, %1" ::"v"(voff), "s"(sbase), "s"(lds) : "memory");
}
DI void dma16nt(const void* sbase, unsigned voff, unsigned lds) {
  asm volatile("s_mov_b32 m0, %2\n\ts_nop 0\n\tglobal_load_lds_dwordx4 %0, %1 nt" ::"v"(voff), "s"(sbase), "s"(lds) : "memory");
}
DI float u2f(unsigned u) { return __builtin_bit_cast(float, u); }
DI unsigned f2u(float f) { return __builtin_bit_cast(unsigned, f); }
DI void flag_set(unsigned addr) {
  FENCE();
  *LDSP(volatile unsigned, addr) = 1u;
  FENCE();
}
DI void flag_wait(unsigned addr) {
  FENCE();
  while (__builtin_amdgcn_readfirstlane(*LDSP(volatile unsigned, addr)) == 0u) __builtin_amdgcn_s_sleep(1);
  FENCE();
}

struct MaskRegs { float t[25]; };
DI void mask_issue(MaskRegs& m, unsigned mb, unsigned mb48) {
  float* t = m.t;
  asm volatile(
      "ds_read_u16_d16_hi %0, %25 offset:0\n ds_read_u16_d16_hi %1, %25 offset:2\n ds_read_u16_d16_hi %2, %25 offset:4\n ds_read_u16_d16_hi %3, %25 offset:6\n"
      "ds_read_u16_d16_hi %4, %25 offset:16\n ds_read_u16_d16_hi %5, %25 offset:18\n ds_read_u16_d16_hi %6, %25 offset:20\n ds_read_u16_d16_hi %7, %25 offset:22\n"
      "ds_read_u16_d16_hi %8, %25 offset:32\n ds_read_u16_d16_hi %9, %25 offset:34\n ds_read_u16_d16_hi %10, %25 offset:36\n ds_read_u16_d16_hi %11, %25 offset:38\n"
      "ds_read_u16_d16_hi %12, %25 offset:48\n ds_read_u16_d16_hi %13, %25 offset:50\n ds_read_u16_d16_hi %14, %25 offset:52\n ds_read_u16_d16_hi %15, %25 offset:54\n"
      "ds_read_u16_d16_hi %16, %25 offset:64\n ds_read_u16_d16_hi %17, %25 offset:66\n ds_read_u16_d16_hi %18, %25 offset:68\n ds_read_u16_d16_hi %19, %25 offset:70\n"
      "ds_read_u16_d16_hi %20, %25 offset:80\n ds_read_u16_d16_hi %21, %25 offset:82\n ds_read_u16_d16_hi %22, %25 offset:84\n ds_read_u16_d16_hi %23, %25 offset:86\n"
      "ds_read_u16_d16_hi %24, %26 offset:96\n"
      : "=&v"(t[0]), "=&v"(t[1]), "=&v"(t[2]), "=&v"(t[3]), "=&v"(t[4]), "=&v"(t[5]), "=&v"(t[6]), "=&v"(t[7]), "=&v"(t[8]), "=&v"(t[9]),
        "=&v"(t[10]), "=&v"(t[11]), "=&v"(t[12]), "=&v"(t[13]), "=&v"(t[14]), "=&v"(t[15]), "=&v"(t[16]), "=&v"(t[17]), "=&v"(t[18]),
        "=&v"(t[19]), "=&v"(t[20]), "=&v"(t[21]), "=&v"(t[22]), "=&v"(t[23]), "=&v"(t[24])
      : "v"(mb), "v"(mb48)
      : "memory");
}
DI void mask_wait(MaskRegs& m, f32x16& m0, f32x16& m1) {
  float* t = m.t;
  asm volatile("s_waitcnt lgkmcnt(0)"
               : "+v"(t[0]), "+v"(t[1]), "+v"(t[2]), "+v"(t[3]), "+v"(t[4]), "+v"(t[5]), "+v"(t[6]), "+v"(t[7]), "+v"(t[8]), "+v"(t[9]),
                 "+v"(t[10]), "+v"(t[11]), "+v"(t[12]), "+v"(t[13]), "+v"(t[14]), "+v"(t[15]), "+v"(t[16]), "+v"(t[17]), "+v"(t[18]),
                 "+v"(t[19]), "+v"(t[20]), "+v"(t[21]), "+v"(t[22]), "+v"(t[23]), "+v"(t[24])
               :
               : "memory");
#pragma unroll
  for (int r = 0; r < 16; ++r) m0[r] = t[r];
#pragma unroll
  for (int r = 0; r < 9; ++r) m1[r] = t[16 + r];
#pragma unroll
  for (int r = 9; r < 16; ++r) m1[r] = t[16];
}

struct Bias { bf16x8 b0, b1, b2; float b48; };
DI Bias bias_load(unsigned bb, int h) {
  Bias r;
  r.b0 = ldsr128(bb + 16 * h); r.b1 = ldsr128(bb + 32 + 16 * h); r.b2 = ldsr128(bb + 64 + 16 * h);
  r.b48 = *LDSP(float, bb + 96 + 4 * h);
  return r;
}
struct SU { f32x16 s0, s1; float b48; };
struct PU { bf16x8 p0, p1, p2, p3; };

DI SU s_unit(bf16x8 k0, bf16x8 k1, bf16x8 k20, bf16x8 k21, bf16x8 qa, bf16x8 qb, const f32x16& m0, const f32x16& m1,
             bf16x8 id0, bf16x8 id1, const Bias& b) {
  SU u;
  u.s0 = mfma(k0, qa, m0);
  u.s1 = mfma(k1, qa, m1);
  u.s0 = mfma(k20, qb, u.s0);
  u.s1 = mfma(k21, qb, u.s1);
  u.s0 = mfma(id0, b.b0, u.s0);
  u.s1 = mfma(id0, b.b2, u.s1);
  u.s0 = mfma(id1, b.b1, u.s0);
  u.b48 = b.b48;
  return u;
}
DI PU softmax_unit(const SU& u) {
  const float LOG2E = 1.4426950408889634f;
  const f32x16& s0 = u.s0; const f32x16& s1 = u.s1;
  const float s48 = s1[8] + u.b48;
  float ma = max3f(s0[0], s0[1], s0[2]), mb = max3f(s0[3], s0[4], s0[5]), mc = max3f(s0[6], s0[7], s0[8]);
  ma = max3f(ma, s0[9], s0[10]); mb = max3f(mb, s0[11], s0[12]); mc = max3f(mc, s0[13], s0[14]);
  ma = max3f(ma, s0[15], s1[0]); mb = max3f(mb, s1[1], s1[2]); mc = max3f(mc, s1[3], s1[4]);
  ma = max3f(ma, s1[5], s1[6]); mb = max3f(mb, s1[7], s48);
  float mx = max3f(ma, mb, mc);
  {
    const unsigned mu = f2u(mx);
    auto sw = __builtin_amdgcn_permlane32_swap(mu, mu, false, false);
    const unsigned w0 = sw[0], w1 = sw[1];
    mx = max2f(u2f(w0), u2f(w1));
  }
  const float mneg = -mx * LOG2E;
  float p0[16], p1[9];
#pragma unroll
  for (int r = 0; r < 16; ++r) p0[r] = __builtin_amdgcn_exp2f(__builtin_fmaf(s0[r], LOG2E, mneg));
#pragma unroll
  for (int r = 0; r < 8; ++r) p1[r] = __builtin_amdgcn_exp2f(__builtin_fmaf(s1[r], LOG2E, mneg));
  p1[8] = __builtin_amdgcn_exp2f(__builtin_fmaf(s48, LOG2E, mneg));
  PU p;
  p.p0 = mk8(cvtpk(p0[0], p0[1]), cvtpk(p0[2], p0[3]), cvtpk(p0[4], p0[5]), cvtpk(p0[6], p0[7]));
  p.p1 = mk8(cvtpk(p0[8], p0[9]), cvtpk(p0[10], p0[11]), cvtpk(p0[12], p0[13]), cvtpk(p0[14], p0[15]));
  p.p2 = mk8(cvtpk(p1[0], p1[1]), cvtpk(p1[2], p1[3]), cvtpk(p1[4], p1[5]), cvtpk(p1[6], p1[7]));
  p.p3 = mk8(cvtpk(p1[8], 0.0f), 0u, 0u, 0u);
  return p;
}
DI f32x16 pv_unit(const bf16x8* v, const PU& p) {
  const f32x16 zero16 = {0, 0, 0, 0, 0, 0, 0, 0, 0, 0, 0, 0, 0, 0, 0, 0};
  f32x16 o = mfma(v[0], p.p0, zero16);
  o = mfma(v[1], p.p1, o);
  o = mfma(v[2], p.p2, o);
  o = mfma(v[3], p.p3, o);
  return o;
}
DI void norm_unit(const f32x16& o, unsigned* og) {
  const float inv = __builtin_amdgcn_rcpf(o[12]);
#pragma unroll
  for (int k = 0; k < 6; ++k) og[k] = cvtpk(o[2 * k] * inv, o[2 * k + 1] * inv);
}

__global__ __launch_bounds__(512, 2) NOPK void swin_attn_kernel(
    const unsigned short* __restrict__ x, const unsigned short* __restrict__ wqkv, const unsigned short* __restrict__ wo,
    const unsigned short* __restrict__ rpb, const int* __restrict__ rp_idx, const unsigned short* __restrict__ mask,
    unsigned short* __restrict__ out
    ) {
  __shared__ __attribute__((aligned(16))) char smem[LDS_TOTAL];
  const unsigned sb = (unsigned)(uintptr_t)LDSP(char, smem);
  const int tid = threadIdx.x;
  const int lane = tid & 63;
  const int wv = __builtin_amdgcn_readfirstlane(tid >> 6);
  const int b = blockIdx.x;
  const int wh = b >> 3, ww0 = (b & 7) * 4;
  const int h = lane >> 5, l31 = lane & 31;
  const int wi = wv >> 1, P = wv & 1;
  const bool late = wv >= 4;
  const unsigned mdelta = ((unsigned)b * 19208u) & 15u;
  const unsigned moff = ((unsigned)b * 19208u) & ~15u;

  const int ww = ww0 + wi;
  const unsigned base0 = (unsigned)(((wh * 7 + 3) * 224 + ww * 7 + 3) * 192);
  const unsigned wrapY = wh == 31 ? 9633792u : 0u, wrapX = ww == 31 ? 43008u : 0u;
  const bool wrap = (wrapY | wrapX) != 0u;
  const unsigned xreg = sb + LDS_X + wi * X_WIN;
  auto issue_x = [&](bool table) NOPK {
#pragma unroll
    for (int jj = 0; jj < 6; ++jj) {
      if (jj < 4 || P == 0) {
        const unsigned i = 384u * P + 64u * jj + lane;
        const unsigned g = ((i * 1366u) >> 16) & 3u;
        const unsigned r = (i * 781u) >> 16;
        unsigned o = r * 41664u + ((i ^ g) << 4);
        if (table | wrap) {
          const unsigned tok = (i * 5462u) >> 16;
          const unsigned f = (r >= 4 ? 1u : 0u) + (tok - 7 * r >= 4 ? 2u : 0u);
          if (table) *LDSP(unsigned, sb + LDS_LP + (6 * P + jj) * 256 + lane * 4) = o + f;
          o -= ((f & 1u) ? wrapY : 0u) + ((f & 2u) ? wrapX : 0u);
        }
        o += base0;
        if (jj < 3 || P == 0 || lane < 12) dma16nt(x, o, xreg + (6 * P + jj) * 1024);
      }
      SB();
    }
  };
  auto issue_m = [&](int w) NOPK {
    const unsigned cs = (mdelta + 4802u * w) >> 4;
#pragma unroll
    for (int rd = 0; rd < 3; ++rd) {
      if (rd < 2 || P == 0) {
        const unsigned c = cs + 64u * (P + 2 * rd);
        unsigned gc = (moff >> 4) + c + lane;
        gc = gc > 307327u ? 307327u : gc;
        dma16nt(mask, gc * 16, sb + LDS_M + c * 16);
      }
    }
  };
  const unsigned wrow = (unsigned)(lane >> 2) * 576u, c4 = lane & 3;

  int ia[5], ib[5], i48 = 0;
  u32x2 rv = {0, 0};
  const int t = tid & 255;
  if (!late) {
    issue_x(false);
    const unsigned dst = sb + LDS_W + P * W_IMG + (3 + wi) * 6144;
    const char* src = (const char*)wqkv + P * 96 + (24 + 3 * wi) * 16;
    if (c4 != 3) {
#pragma unroll
      for (int rb = 0; rb < 6; ++rb) dma16(src + rb * 9216, wrow + c4 * 16, dst + rb * 1024);
    } else {
#pragma unroll
      for (int rb = 0; rb < 6; ++rb) *LDSP(u32x4, dst + rb * 1024 + lane * 16) = u32x4{0, 0, 0, 0};
    }
    if (tid < 32) *LDSP(unsigned, sb + LDS_FLAG + tid * 4) = 0u;
#pragma unroll
    for (int j = 0; j < 5; ++j) {
      if (j < 4 || P == 1) {
        const int pc = (wi * 9 + P * 4) + j;
        dma16((const char*)wo + pc * 1024, lane * 16, sb + LDS_WO + pc * 1024);
      }
    }
    STAMP(2);
    if (P == 0) WAIT_VM(4); else WAIT_VM(5);
    WAIT_LGKM0();
  } else {
    const int half = wi - 2;
    {
      const unsigned v01 = wrow + (c4 + (c4 >> 1)) * 16;
      const unsigned v2 = half ? wrow + (2 + 3 * (c4 & 1) + 12 * (c4 >> 1)) * 16 : v01;
      const char* src = (const char*)wqkv + P * 96;
      const unsigned dst = sb + LDS_W + P * W_IMG;
#pragma unroll
      for (int rb = 0; rb < 6; ++rb) dma16(src + rb * 9216, v2, dst + half * 2 * 6144 + rb * 1024);
#pragma unroll
      for (int k = 0; k < 3; ++k) dma16(src + 192 + (3 * half + k) * 9216, v01, dst + 6144 + (3 * half + k) * 1024);
    }
    if (t < 169) asm volatile("global_load_dwordx2 %0, %1, %2" : "=v"(rv) : "v"(t * 8), "s"(rpb) : "memory");
#pragma unroll
    for (int rd = 0; rd < 5; ++rd) {
      const int e = t + 256 * rd;
      const int q = (e * 2731) >> 16, kp = e - 24 * q;
      int o = (q * 49 + 2 * kp) * 4;
      o = e < 1176 ? o : 0;
      asm volatile("global_load_dword %0, %2, %3\n\tglobal_load_dword %1, %2, %3 offset:4" : "=&v"(ia[rd]), "=&v"(ib[rd]) : "v"(o), "s"(rp_idx) : "memory");
    }
    {
      const int o = t < 49 ? (t * 49 + 48) * 4 : 0;
      asm volatile("global_load_dword %0, %1, %2" : "=v"(i48) : "v"(o), "s"(rp_idx) : "memory");
    }
    issue_m(half);
    issue_x(half == 0);
    STAMP(2);
    if (P == 0) WAIT_VM(9); else WAIT_VM(6);
    asm volatile("" : "+v"(rv), "+v"(i48));
#pragma unroll
    for (int rd = 0; rd < 5; ++rd) asm volatile("" : "+v"(ia[rd]), "+v"(ib[rd]));
    if (t < 169) *LDSP(u32x2, sb + LDS_RPB + t * 8) = rv;
    WAIT_LGKM0();
  }
  FENCE();
  __builtin_amdgcn_s_barrier();
  FENCE();
  STAMP(3);
  if (late) {
    issue_m(wi);
#pragma unroll
    for (int rd = 0; rd < 5; ++rd) {
      const int e = t + 256 * rd;
      const int q = (e * 2731) >> 16, kp = e - 24 * q;
      if (e < 1176) {
        const u32x2 va = *LDSP(u32x2, sb + LDS_RPB + ia[rd] * 8);
        const u32x2 vb = *LDSP(u32x2, sb + LDS_RPB + ib[rd] * 8);
        const unsigned dst = sb + LDS_B + q * 112 + kp * 4;
        *LDSP(unsigned, dst + 0 * 5488) = __builtin_amdgcn_perm(vb[0], va[0], 0x05040100u);
        *LDSP(unsigned, dst + 1 * 5488) = __builtin_amdgcn_perm(vb[0], va[0], 0x07060302u);
        *LDSP(unsigned, dst + 2 * 5488) = __builtin_amdgcn_perm(vb[1], va[1], 0x05040100u);
        *LDSP(unsigned, dst + 3 * 5488) = __builtin_amdgcn_perm(vb[1], va[1], 0x07060302u);
      }
    }
    if (t < 49) {
      const u32x2 va = *LDSP(u32x2, sb + LDS_RPB + i48 * 8);
      const unsigned dst = sb + LDS_B + t * 112 + 96;
      *LDSP(u32x2, dst + 0 * 5488) = u32x2{va[0] << 16, 0xff800000u};
      *LDSP(u32x2, dst + 1 * 5488) = u32x2{va[0] & 0xffff0000u, 0xff800000u};
      *LDSP(u32x2, dst + 2 * 5488) = u32x2{va[1] << 16, 0xff800000u};
      *LDSP(u32x2, dst + 3 * 5488) = u32x2{va[1] & 0xffff0000u, 0xff800000u};
    }
    STAMP(11);
    if (P == 0) WAIT_VM(9); else WAIT_VM(6);
    WAIT_LGKM0();
    FENCE();
    __builtin_amdgcn_s_barrier();
    FENCE();
    STAMP(12);
    if (P == 0) WAIT_VM(3); else WAIT_VM(2);
    flag_set(sb + LDS_FLAG + (wv * 4 + 0) * 4);
    flag_wait(sb + LDS_FLAG + ((wv ^ 1) * 4 + 0) * 4);
  }
  STAMP(4);

  bf16x8 xf[2][6];
  {
#pragma unroll
    for (int u = 0; u < 2; ++u) {
      int tok = 32 * u + l31;
      tok = tok > 48 ? 48 : tok;
      const unsigned base = xreg + tok * 192;
      const unsigned sw = (unsigned)(h ^ ((tok >> 2) & 3));
      const unsigned aE = base + sw * 16, aO = base + (sw ^ 2) * 16;
#pragma unroll
      for (int s = 0; s < 6; ++s) xf[u][s] = ldsr128(((s & 1) ? aO : aE) + (s >> 1) * 64);
    }
  }
  const int li = lane & 15, tq = li >> 2, tp = li & 3, g1 = (lane >> 4) & 1;
  const unsigned wbase = sb + LDS_W + P * W_IMG + (8 * h + tq) * 64 + g1 * 32 + tp * 8;

  bf16x8 qf01[2][2], qf2[2][2];
  bf16x8 kf01[2][2], kf22[2];
  bf16x8 vf[2][4];
  const float qscale = 0.20412414523193154f;
  const f32x16 zero16 = {0, 0, 0, 0, 0, 0, 0, 0, 0, 0, 0, 0, 0, 0, 0, 0};
  {
#pragma unroll
    for (int T = 0; T < 3; ++T) {
      f32x16 a0 = zero16, a1 = zero16;
#pragma unroll
      for (int s = 0; s < 6; ++s) {
        const bf16x8 wf = tr2(wbase + T * 6144 + s * 1024, wbase + T * 6144 + s * 1024 + 256);
        a0 = mfma(wf, xf[0][s], a0);
        a1 = mfma(wf, xf[1][s], a1);
      }
      if (T == 0) {
#pragma unroll
        for (int r = 0; r < 16; ++r) { a0[r] *= qscale; a1[r] *= qscale; }
        qf01[0][0] = pack8(a0, 0); qf01[0][1] = pack8(a0, 8);
        qf01[1][0] = pack8(a1, 0); qf01[1][1] = pack8(a1, 8);
      } else if (T == 1) {
        kf01[0][0] = pack8(a0, 0); kf01[0][1] = pack8(a0, 8);
        kf01[1][0] = pack8(a1, 0); kf01[1][1] = pack8(a1, 8);
      } else {
#pragma unroll
        for (int r = 0; r < 8; ++r) { a0[r] *= qscale; a1[r] *= qscale; }
        qf2[0][0] = mk8(cvtpk(a0[0], a0[1]), cvtpk(a0[2], a0[3]), 0u, 0u);
        qf2[0][1] = mk8(0u, 0u, cvtpk(a0[4], a0[5]), cvtpk(a0[6], a0[7]));
        qf2[1][0] = mk8(cvtpk(a1[0], a1[1]), cvtpk(a1[2], a1[3]), 0u, 0u);
        qf2[1][1] = mk8(0u, 0u, cvtpk(a1[4], a1[5]), cvtpk(a1[6], a1[7]));
        kf22[0] = pack8(a0, 8);
        kf22[1] = pack8(a1, 8);
      }
    }
    f32x16 cone;
    {
      const float cv = l31 >= 24 ? 1.0f : 0.0f;
#pragma unroll
      for (int r = 0; r < 16; ++r) cone[r] = cv;
    }
#pragma unroll
    for (int hd = 0; hd < 2; ++hd) {
      f32x16 a0 = cone, a1 = cone;
#pragma unroll
      for (int s = 0; s < 6; ++s) {
        const bf16x8 wf = tr2(wbase + (3 + hd) * 6144 + s * 1024, wbase + (3 + hd) * 6144 + s * 1024 + 256);
        a0 = mfma(xf[0][s], wf, a0);
        a1 = mfma(xf[1][s], wf, a1);
      }
      vf[hd][0] = pack8(a0, 0);
      vf[hd][1] = pack8(a0, 8);
      vf[hd][2] = pack8(a1, 0);
      vf[hd][3] = mk8(cvtpk(a1[8], a1[9]), 0u, 0u, 0u);
    }
  }
  STAMP(5);
  WAIT_VM(0);
  if (late) {
    flag_set(sb + LDS_FLAG + (wv * 4 + 1) * 4);
    flag_wait(sb + LDS_FLAG + ((wv ^ 1) * 4 + 1) * 4);
  } else {
    FENCE();
    __builtin_amdgcn_s_barrier();
    FENCE();
  }
  STAMP(6);

  bf16x8 id0, id1;
  {
    const int d0 = l31 - 8 * h, d1 = l31 - 16 - 8 * h;
    unsigned a[4], c[4];
#pragma unroll
    for (int k = 0; k < 4; ++k) {
      a[k] = (d0 == 2 * k ? 0x3f80u : 0u) | (d0 == 2 * k + 1 ? 0x3f800000u : 0u);
      c[k] = (d1 == 2 * k ? 0x3f80u : 0u) | (d1 == 2 * k + 1 ? 0x3f800000u : 0u);
    }
    id0 = mk8(a[0], a[1], a[2], a[3]);
    id1 = mk8(c[0], c[1], c[2], c[3]);
  }
  const int q1 = 32 + l31 > 48 ? 48 : 32 + l31;
  const unsigned mbase = sb + LDS_M + mdelta + wi * 4802;
  const unsigned bbase = sb + LDS_B + (2 * P) * 5488;
  const unsigned exb = xreg + lane * 16;

  MaskRegs mr;
  f32x16 m0, m1;
  unsigned ogA[6], ogB[6];
  mask_issue(mr, mbase + l31 * 98 + 8 * h, mbase + l31 * 98);
  Bias bi0 = bias_load(bbase + l31 * 112, h);
  Bias bi1 = bias_load(bbase + 5488 + l31 * 112, h);
  mask_wait(mr, m0, m1);
  SB();
  SU su0 = s_unit(kf01[0][0], kf01[1][0], kf22[0], kf22[1], qf01[0][0], qf2[0][0], m0, m1, id0, id1, bi0);
  SB();
  SU su1 = s_unit(kf01[0][1], kf01[1][1], kf22[0], kf22[1], qf01[0][1], qf2[0][1], m0, m1, id0, id1, bi1);
  PU pu0 = softmax_unit(su0);
  Bias bi2 = bias_load(bbase + q1 * 112, h);
  SB();
  mask_issue(mr, mbase + q1 * 98 + 8 * h, mbase + q1 * 98);
  f32x16 o0 = pv_unit(vf[0], pu0);
  Bias bi3 = bias_load(bbase + 5488 + q1 * 112, h);
  mask_wait(mr, m0, m1);
  SB();
  SU su2 = s_unit(kf01[0][0], kf01[1][0], kf22[0], kf22[1], qf01[1][0], qf2[1][0], m0, m1, id0, id1, bi2);
  PU pu1 = softmax_unit(su1);
  norm_unit(o0, ogA);
  SB();
  f32x16 o1 = pv_unit(vf[1], pu1);
  SU su3 = s_unit(kf01[0][1], kf01[1][1], kf22[0], kf22[1], qf01[1][1], qf2[1][1], m0, m1, id0, id1, bi3);
  PU pu2 = softmax_unit(su2);
  norm_unit(o1, ogB);
  *LDSP(bf16x8, exb + P * 3072) = mk8(ogA[0], ogA[1], ogA[2], ogA[3]);
  *LDSP(bf16x8, exb + P * 3072 + 1024) = mk8(ogA[4], ogA[5], ogB[0], ogB[1]);
  *LDSP(bf16x8, exb + P * 3072 + 2048) = mk8(ogB[2], ogB[3], ogB[4], ogB[5]);
  SB();
  WAIT_LGKM0();
  flag_set(sb + LDS_FLAG + (wv * 4 + 2) * 4);
  SB();
  f32x16 o2 = pv_unit(vf[0], pu2);
  PU pu3 = softmax_unit(su3);
  norm_unit(o2, ogA);
  SB();
  f32x16 o3 = pv_unit(vf[1], pu3);
  norm_unit(o3, ogB);
  const bf16x8 t1f0 = mk8(ogA[0], ogA[1], ogA[2], ogA[3]);
  const bf16x8 t1f1 = mk8(ogA[4], ogA[5], ogB[0], ogB[1]);
  const bf16x8 t1f2 = mk8(ogB[2], ogB[3], ogB[4], ogB[5]);
  STAMP(7);

  const unsigned wob = sb + LDS_WO + (4 * h + tq) * 192 + (16 * g1 + 8 * (tp & 1) + 4 * (tp >> 1)) * 2;
  f32x16 acc[3] = {zero16, zero16, zero16};
  if (P == 0) {
    *LDSP(bf16x8, exb + 6144) = t1f0;
    *LDSP(bf16x8, exb + 6144 + 1024) = t1f1;
    *LDSP(bf16x8, exb + 6144 + 2048) = t1f2;
    WAIT_LGKM0();
    flag_set(sb + LDS_FLAG + (wv * 4 + 3) * 4);
    flag_wait(sb + LDS_FLAG + ((wv ^ 1) * 4 + 2) * 4);
#pragma unroll
    for (int s = 0; s < 6; ++s) {
      const bf16x8 bfr = ldsr128(exb + s * 1024);
#pragma unroll
      for (int T = 0; T < 3; ++T)
        acc[T] = mfma(tr2(wob + (16 * s) * 192 + 64 * T, wob + (16 * s + 8) * 192 + 64 * T), bfr, acc[T]);
    }
  } else {
#pragma unroll
    for (int s = 3; s < 6; ++s) {
      const bf16x8 bfr = s == 3 ? t1f0 : (s == 4 ? t1f1 : t1f2);
#pragma unroll
      for (int T = 0; T < 3; ++T)
        acc[T] = mfma(tr2(wob + (16 * s) * 192 + 64 * T, wob + (16 * s + 8) * 192 + 64 * T), bfr, acc[T]);
    }
    flag_wait(sb + LDS_FLAG + ((wv ^ 1) * 4 + 3) * 4);
#pragma unroll
    for (int s = 0; s < 3; ++s) {
      const bf16x8 bfr = ldsr128(exb + 6144 + s * 1024);
#pragma unroll
      for (int T = 0; T < 3; ++T)
        acc[T] = mfma(tr2(wob + (16 * s) * 192 + 64 * T, wob + (16 * s + 8) * 192 + 64 * T), bfr, acc[T]);
    }
  }
  STAMP(8);
  {
    int tok = 32 * P + l31;
    const bool tv = tok < 49;
    tok = tv ? tok : 48;
    const unsigned g = (unsigned)((tok >> 2) & 3);
    const unsigned base = xreg + tok * 192;
    const unsigned a0 = base + ((h ^ g) << 4), a1 = base + (((2 + h) ^ g) << 4);
    if (tv) {
#pragma unroll
      for (int T = 0; T < 3; ++T) {
        *LDSP(bf16x8, a0 + 64 * T) = pack8(acc[T], 0);
        *LDSP(bf16x8, a1 + 64 * T) = pack8(acc[T], 8);
      }
    }
    const unsigned rb = xreg + P * 6144 + lane * 16;
    const unsigned lpb = sb + LDS_LP + P * 1536 + lane * 4;
#pragma unroll
    for (int jj = 0; jj < 6; ++jj) {
      if (jj < 3 || P == 0 || (jj == 3 && lane < 12)) {
        const unsigned lp = *LDSP(unsigned, lpb + jj * 256);
        unsigned o = base0 + (lp & ~15u);
        if (wrap) o -= ((lp & 1u) ? wrapY : 0u) + ((lp & 2u) ? wrapX : 0u);
        const bf16x8 vv = ldsr128(rb + jj * 1024);
        asm volatile("global_store_dwordx4 %0, %1, %2 sc1\n\ts_nop 1" ::"v"(o), "v"(vv), "s"(out) : "memory");
      }
    }
  }
}

extern "C" void kernel_launch(void* const* d_in, const int* in_sizes, int n_in, void* d_out, int out_size, void* d_ws,
                              size_t ws_size, hipStream_t stream) {
  swin_attn_kernel<<<256, 512, 0, stream>>>((const unsigned short*)d_in[0], (const unsigned short*)d_in[1],
                                            (const unsigned short*)d_in[2], (const unsigned short*)d_in[3],
                                            (const int*)d_in[4], (const unsigned short*)d_in[5], (unsigned short*)d_out
  );
}
```

```cpp
#include <hip/hip_runtime.h>
#include <stdint.h>

typedef float f32x16 __attribute__((ext_vector_type(16)));
typedef float f32x2 __attribute__((ext_vector_type(2)));
typedef __bf16 bf16x8 __attribute__((ext_vector_type(8)));
typedef __bf16 bf16x2 __attribute__((ext_vector_type(2)));
typedef short s16x4 __attribute__((ext_vector_type(4)));
typedef unsigned u32x4 __attribute__((ext_vector_type(4)));
typedef unsigned u32x2 __attribute__((ext_vector_type(2)));

#if defined(__HIP_DEVICE_COMPILE__)
#define NOPK __attribute__((target("no-packed-fp32-ops")))
#else
#define NOPK
#endif
#define DI __device__ __forceinline__ NOPK
#define LDSP(T, a) ((__attribute__((address_space(3))) T*)(a))

constexpr int LDS_W = 0;
constexpr int W_IMG = 5 * 6144;
constexpr int LDS_WO = 2 * W_IMG;
constexpr int LDS_X = LDS_WO + 18432;
constexpr int X_WIN = 49 * 192;
constexpr int LDS_M = LDS_X + 4 * X_WIN;
constexpr int LDS_B = LDS_M + 1224 * 16;
constexpr int LDS_RPB = LDS_B + 4 * 49 * 112;
constexpr int LDS_FLAG = LDS_RPB + 1360;
constexpr int LDS_LP = LDS_FLAG + 128;
constexpr int LDS_TOTAL = LDS_LP + 2560;

#define WAIT_VM(n) __builtin_amdgcn_s_waitcnt(0x0F70 | ((n) & 15) | (((n) >> 4) << 14))
#define WAIT_LGKM0() __builtin_amdgcn_s_waitcnt(0xC07F)
#define FENCE() asm volatile("" ::: "memory")

DI unsigned cvtpk(float lo, float hi) {
  f32x2 v = {lo, hi};
  bf16x2 b = __builtin_convertvector(v, bf16x2);
  return __builtin_bit_cast(unsigned, b);
}
DI float max2f(float a, float b) { return __builtin_elementwise_maximum(a, b); }
DI float max3f(float a, float b, float c) { return max2f(max2f(a, b), c); }
DI bf16x8 mk8(unsigned a, unsigned b, unsigned c, unsigned d) {
  u32x4 v = {a, b, c, d};
  return __builtin_bit_cast(bf16x8, v);
}
DI bf16x8 pack8(const f32x16& a, int r0) {
  return mk8(cvtpk(a[r0], a[r0 + 1]), cvtpk(a[r0 + 2], a[r0 + 3]), cvtpk(a[r0 + 4], a[r0 + 5]), cvtpk(a[r0 + 6], a[r0 + 7]));
}
DI f32x16 mfma(bf16x8 a, bf16x8 b, f32x16 c) { return __builtin_amdgcn_mfma_f32_32x32x16_bf16(a, b, c, 0, 0, 0); }

DI bf16x8 tr2(unsigned addr0, unsigned addr1) {
  s16x4 lo = __builtin_amdgcn_ds_read_tr16_b64_v4i16(LDSP(s16x4, addr0));
  s16x4 hi = __builtin_amdgcn_ds_read_tr16_b64_v4i16(LDSP(s16x4, addr1));
  u32x2 a = __builtin_bit_cast(u32x2, lo), b = __builtin_bit_cast(u32x2, hi);
  return mk8(a[0], a[1], b[0], b[1]);
}
DI bf16x8 ldsr128(unsigned addr) { return *LDSP(bf16x8, addr); }

#define STAMP(k)
#define SB() __builtin_amdgcn_sched_barrier(0)

DI void dma16(const void* sbase, unsigned voff, unsigned lds) {
  asm volatile("s_mov_b32 m0, %2\n\ts_nop 0\n\tglobal_load_lds_dwordx4 %0, %1" ::"v"(voff), "s"(sbase), "s"(lds) : "memory");
}
DI void dma16nt(const void* sbase, unsigned voff, unsigned lds) {
  asm volatile("s_mov_b32 m0, %2\n\ts_nop 0\n\tglobal_load_lds_dwordx4 %0, %1 nt" ::"v"(voff), "s"(sbase), "s"(lds) : "memory");
}
DI float u2f(unsigned u) { return __builtin_bit_cast(float, u); }
DI unsigned f2u(float f) { return __builtin_bit_cast(unsigned, f); }
DI void flag_set(unsigned addr) {
  FENCE();
  *LDSP(volatile unsigned, addr) = 1u;
  FENCE();
}
DI void flag_wait(unsigned addr) {
  FENCE();
  while (__builtin_amdgcn_readfirstlane(*LDSP(volatile unsigned, addr)) == 0u) __builtin_amdgcn_s_sleep(1);
  FENCE();
}

struct MaskRegs { float t[25]; };
DI void mask_issue(MaskRegs& m, unsigned mb, unsigned mb48) {
  float* t = m.t;
  asm volatile(
      "ds_read_u16_d16_hi %0, %25 offset:0\n ds_read_u16_d16_hi %1, %25 offset:2\n ds_read_u16_d16_hi %2, %25 offset:4\n ds_read_u16_d16_hi %3, %25 offset:6\n"
      "ds_read_u16_d16_hi %4, %25 offset:16\n ds_read_u16_d16_hi %5, %25 offset:18\n ds_read_u16_d16_hi %6, %25 offset:20\n ds_read_u16_d16_hi %7, %25 offset:22\n"
      "ds_read_u16_d16_hi %8, %25 offset:32\n ds_read_u16_d16_hi %9, %25 offset:34\n ds_read_u16_d16_hi %10, %25 offset:36\n ds_read_u16_d16_hi %11, %25 offset:38\n"
      "ds_read_u16_d16_hi %12, %25 offset:48\n ds_read_u16_d16_hi %13, %25 offset:50\n ds_read_u16_d16_hi %14, %25 offset:52\n ds_read_u16_d16_hi %15, %25 offset:54\n"
      "ds_read_u16_d16_hi %16, %25 offset:64\n ds_read_u16_d16_hi %17, %25 offset:66\n ds_read_u16_d16_hi %18, %25 offset:68\n ds_read_u16_d16_hi %19, %25 offset:70\n"
      "ds_read_u16_d16_hi %20, %25 offset:80\n ds_read_u16_d16_hi %21, %25 offset:82\n ds_read_u16_d16_hi %22, %25 offset:84\n ds_read_u16_d16_hi %23, %25 offset:86\n"
      "ds_read_u16_d16_hi %24, %26 offset:96\n"
      : "=&v"(t[0]), "=&v"(t[1]), "=&v"(t[2]), "=&v"(t[3]), "=&v"(t[4]), "=&v"(t[5]), "=&v"(t[6]), "=&v"(t[7]), "=&v"(t[8]), "=&v"(t[9]),
        "=&v"(t[10]), "=&v"(t[11]), "=&v"(t[12]), "=&v"(t[13]), "=&v"(t[14]), "=&v"(t[15]), "=&v"(t[16]), "=&v"(t[17]), "=&v"(t[18]),
        "=&v"(t[19]), "=&v"(t[20]), "=&v"(t[21]), "=&v"(t[22]), "=&v"(t[23]), "=&v"(t[24])
      : "v"(mb), "v"(mb48)
      : "memory");
}
DI void mask_wait(MaskRegs& m, f32x16& m0, f32x16& m1) {
  float* t = m.t;
  asm volatile("s_waitcnt lgkmcnt(0)"
               : "+v"(t[0]), "+v"(t[1]), "+v"(t[2]), "+v"(t[3]), "+v"(t[4]), "+v"(t[5]), "+v"(t[6]), "+v"(t[7]), "+v"(t[8]), "+v"(t[9]),
                 "+v"(t[10]), "+v"(t[11]), "+v"(t[12]), "+v"(t[13]), "+v"(t[14]), "+v"(t[15]), "+v"(t[16]), "+v"(t[17]), "+v"(t[18]),
                 "+v"(t[19]), "+v"(t[20]), "+v"(t[21]), "+v"(t[22]), "+v"(t[23]), "+v"(t[24])
               :
               : "memory");
#pragma unroll
  for (int r = 0; r < 16; ++r) m0[r] = t[r];
#pragma unroll
  for (int r = 0; r < 9; ++r) m1[r] = t[16 + r];
#pragma unroll
  for (int r = 9; r < 16; ++r) m1[r] = t[16];
}

struct Bias { bf16x8 b0, b1, b2; float b48; };
DI Bias bias_load(unsigned bb, int h) {
  Bias r;
  r.b0 = ldsr128(bb + 16 * h); r.b1 = ldsr128(bb + 32 + 16 * h); r.b2 = ldsr128(bb + 64 + 16 * h);
  r.b48 = *LDSP(float, bb + 96 + 4 * h);
  return r;
}
struct SU { f32x16 s0, s1; float b48; };
struct PU { bf16x8 p0, p1, p2, p3; };

DI SU s_unit(bf16x8 k0, bf16x8 k1, bf16x8 k20, bf16x8 k21, bf16x8 qa, bf16x8 qb, const f32x16& m0, const f32x16& m1,
             bf16x8 id0, bf16x8 id1, const Bias& b) {
  SU u;
  u.s0 = mfma(k0, qa, m0);
  u.s1 = mfma(k1, qa, m1);
  u.s0 = mfma(k20, qb, u.s0);
  u.s1 = mfma(k21, qb, u.s1);
  u.s0 = mfma(id0, b.b0, u.s0);
  u.s1 = mfma(id0, b.b2, u.s1);
  u.s0 = mfma(id1, b.b1, u.s0);
  u.b48 = b.b48;
  return u;
}
DI PU softmax_unit(const SU& u) {
  const float LOG2E = 1.4426950408889634f;
  const f32x16& s0 = u.s0; const f32x16& s1 = u.s1;
  const float s48 = s1[8] + u.b48;
  float ma = max3f(s0[0], s0[1], s0[2]), mb = max3f(s0[3], s0[4], s0[5]), mc = max3f(s0[6], s0[7], s0[8]);
  ma = max3f(ma, s0[9], s0[10]); mb = max3f(mb, s0[11], s0[12]); mc = max3f(mc, s0[13], s0[14]);
  ma = max3f(ma, s0[15], s1[0]); mb = max3f(mb, s1[1], s1[2]); mc = max3f(mc, s1[3], s1[4]);
  ma = max3f(ma, s1[5], s1[6]); mb = max3f(mb, s1[7], s48);
  float mx = max3f(ma, mb, mc);
  {
    const unsigned mu = f2u(mx);
    auto sw = __builtin_amdgcn_permlane32_swap(mu, mu, false, false);
    const unsigned w0 = sw[0], w1 = sw[1];
    mx = max2f(u2f(w0), u2f(w1));
  }
  const float mneg = -mx * LOG2E;
  float p0[16], p1[9];
#pragma unroll
  for (int r = 0; r < 16; ++r) p0[r] = __builtin_amdgcn_exp2f(__builtin_fmaf(s0[r], LOG2E, mneg));
#pragma unroll
  for (int r = 0; r < 8; ++r) p1[r] = __builtin_amdgcn_exp2f(__builtin_fmaf(s1[r], LOG2E, mneg));
  p1[8] = __builtin_amdgcn_exp2f(__builtin_fmaf(s48, LOG2E, mneg));
  PU p;
  p.p0 = mk8(cvtpk(p0[0], p0[1]), cvtpk(p0[2], p0[3]), cvtpk(p0[4], p0[5]), cvtpk(p0[6], p0[7]));
  p.p1 = mk8(cvtpk(p0[8], p0[9]), cvtpk(p0[10], p0[11]), cvtpk(p0[12], p0[13]), cvtpk(p0[14], p0[15]));
  p.p2 = mk8(cvtpk(p1[0], p1[1]), cvtpk(p1[2], p1[3]), cvtpk(p1[4], p1[5]), cvtpk(p1[6], p1[7]));
  p.p3 = mk8(cvtpk(p1[8], 0.0f), 0u, 0u, 0u);
  return p;
}
DI f32x16 pv_unit(const bf16x8* v, const PU& p) {
  const f32x16 zero16 = {0, 0, 0, 0, 0, 0, 0, 0, 0, 0, 0, 0, 0, 0, 0, 0};
  f32x16 o = mfma(v[0], p.p0, zero16);
  o = mfma(v[1], p.p1, o);
  o = mfma(v[2], p.p2, o);
  o = mfma(v[3], p.p3, o);
  return o;
}
DI void norm_unit(const f32x16& o, unsigned* og) {
  const float inv = __builtin_amdgcn_rcpf(o[12]);
#pragma unroll
  for (int k = 0; k < 6; ++k) og[k] = cvtpk(o[2 * k] * inv, o[2 * k + 1] * inv);
}

__global__ __launch_bounds__(512, 2) NOPK void swin_attn_kernel(
    const unsigned short* __restrict__ x, const unsigned short* __restrict__ wqkv, const unsigned short* __restrict__ wo,
    const unsigned short* __restrict__ rpb, const int* __restrict__ rp_idx, const unsigned short* __restrict__ mask,
    unsigned short* __restrict__ out
    ) {
  __shared__ __attribute__((aligned(16))) char smem[LDS_TOTAL];
  const unsigned sb = (unsigned)(uintptr_t)LDSP(char, smem);
  const int tid = threadIdx.x;
  const int lane = tid & 63;
  const int wv = __builtin_amdgcn_readfirstlane(tid >> 6);
  const int b = blockIdx.x;
  const int wh = b >> 3, ww0 = (b & 7) * 4;
  const int h = lane >> 5, l31 = lane & 31;
  const int wi = wv >> 1, P = wv & 1;
  const bool late = wv >= 4;
  const unsigned mdelta = ((unsigned)b * 19208u) & 15u;
  const unsigned moff = ((unsigned)b * 19208u) & ~15u;

  const int ww = ww0 + wi;
  const unsigned base0 = (unsigned)(((wh * 7 + 3) * 224 + ww * 7 + 3) * 192);
  const unsigned wrapY = wh == 31 ? 9633792u : 0u, wrapX = ww == 31 ? 43008u : 0u;
  const bool wrap = (wrapY | wrapX) != 0u;
  const unsigned xreg = sb + LDS_X + wi * X_WIN;
  auto issue_x = [&](bool table) NOPK {
#pragma unroll
    for (int jj = 0; jj < 6; ++jj) {
      if (jj < 4 || P == 0) {
        const unsigned i = 384u * P + 64u * jj + lane;
        const unsigned g = ((i * 1366u) >> 16) & 3u;
        const unsigned r = (i * 781u) >> 16;
        unsigned o = r * 41664u + ((i ^ g) << 4);
        if (table | wrap) {
          const unsigned tok = (i * 5462u) >> 16;
          const unsigned f = (r >= 4 ? 1u : 0u) + (tok - 7 * r >= 4 ? 2u : 0u);
          if (table) *LDSP(unsigned, sb + LDS_LP + (6 * P + jj) * 256 + lane * 4) = o + f;
          o -= ((f & 1u) ? wrapY : 0u) + ((f & 2u) ? wrapX : 0u);
        }
        o += base0;
        if (jj < 3 || P == 0 || lane < 12) dma16nt(x, o, xreg + (6 * P + jj) * 1024);
      }
      SB();
    }
  };
  auto issue_m = [&](int w) NOPK {
    const unsigned cs = (mdelta + 4802u * w) >> 4;
#pragma unroll
    for (int rd = 0; rd < 3; ++rd) {
      if (rd < 2 || P == 0) {
        const unsigned c = cs + 64u * (P + 2 * rd);
        unsigned gc = (moff >> 4) + c + lane;
        gc = gc > 307327u ? 307327u : gc;
        dma16nt(mask, gc * 16, sb + LDS_M + c * 16);
      }
    }
  };
  const unsigned wrow = (unsigned)(lane >> 2) * 576u, c4 = lane & 3;

  int ia[5], ib[5], i48 = 0;
  u32x2 rv = {0, 0};
  const int t = tid & 255;
  if (!late) {
    issue_x(false);
    const unsigned dst = sb + LDS_W + P * W_IMG + (3 + wi) * 6144;
    const char* src = (const char*)wqkv + P * 96 + (24 + 3 * wi) * 16;
    if (c4 != 3) {
#pragma unroll
      for (int rb = 0; rb < 6; ++rb) dma16(src + rb * 9216, wrow + c4 * 16, dst + rb * 1024);
    } else {
#pragma unroll
      for (int rb = 0; rb < 6; ++rb) *LDSP(u32x4, dst + rb * 1024 + lane * 16) = u32x4{0, 0, 0, 0};
    }
    if (tid < 32) *LDSP(unsigned, sb + LDS_FLAG + tid * 4) = 0u;
#pragma unroll
    for (int j = 0; j < 5; ++j) {
      if (j < 4 || P == 1) {
        const int pc = (wi * 9 + P * 4) + j;
        dma16((const char*)wo + pc * 1024, lane * 16, sb + LDS_WO + pc * 1024);
      }
    }
    STAMP(2);
    if (P == 0) WAIT_VM(4); else WAIT_VM(5);
    WAIT_LGKM0();
  } else {
    const int half = wi - 2;
    {
      const unsigned long long pc = __builtin_amdgcn_s_getpc();
      const unsigned co = ((unsigned)(wv - 4) * 64u + (unsigned)lane) * 64u;
      asm volatile("s_mov_b32 m0, %2\n\ts_nop 0\n\tglobal_load_lds_dword %0, %1" ::"v"(co), "s"(pc), "s"(sb + LDS_B + (wv - 4) * 256) : "memory");
    }
    {
      const unsigned v01 = wrow + (c4 + (c4 >> 1)) * 16;
      const unsigned v2 = half ? wrow + (2 + 3 * (c4 & 1) + 12 * (c4 >> 1)) * 16 : v01;
      const char* src = (const char*)wqkv + P * 96;
      const unsigned dst = sb + LDS_W + P * W_IMG;
#pragma unroll
      for (int rb = 0; rb < 6; ++rb) dma16(src + rb * 9216, v2, dst + half * 2 * 6144 + rb * 1024);
#pragma unroll
      for (int k = 0; k < 3; ++k) dma16(src + 192 + (3 * half + k) * 9216, v01, dst + 6144 + (3 * half + k) * 1024);
    }
    if (t < 169) asm volatile("global_load_dwordx2 %0, %1, %2" : "=v"(rv) : "v"(t * 8), "s"(rpb) : "memory");
#pragma unroll
    for (int rd = 0; rd < 5; ++rd) {
      const int e = t + 256 * rd;
      const int q = (e * 2731) >> 16, kp = e - 24 * q;
      int o = (q * 49 + 2 * kp) * 4;
      o = e < 1176 ? o : 0;
      asm volatile("global_load_dword %0, %2, %3\n\tglobal_load_dword %1, %2, %3 offset:4" : "=&v"(ia[rd]), "=&v"(ib[rd]) : "v"(o), "s"(rp_idx) : "memory");
    }
    {
      const int o = t < 49 ? (t * 49 + 48) * 4 : 0;
      asm volatile("global_load_dword %0, %1, %2" : "=v"(i48) : "v"(o), "s"(rp_idx) : "memory");
    }
    issue_m(half);
    issue_x(half == 0);
    STAMP(2);
    if (P == 0) WAIT_VM(9); else WAIT_VM(6);
    asm volatile("" : "+v"(rv), "+v"(i48));
#pragma unroll
    for (int rd = 0; rd < 5; ++rd) asm volatile("" : "+v"(ia[rd]), "+v"(ib[rd]));
    if (t < 169) *LDSP(u32x2, sb + LDS_RPB + t * 8) = rv;
    WAIT_LGKM0();
  }
  FENCE();
  __builtin_amdgcn_s_barrier();
  FENCE();
  STAMP(3);
  if (late) {
    issue_m(wi);
#pragma unroll
    for (int rd = 0; rd < 5; ++rd) {
      const int e = t + 256 * rd;
      const int q = (e * 2731) >> 16, kp = e - 24 * q;
      if (e < 1176) {
        const u32x2 va = *LDSP(u32x2, sb + LDS_RPB + ia[rd] * 8);
        const u32x2 vb = *LDSP(u32x2, sb + LDS_RPB + ib[rd] * 8);
        const unsigned dst = sb + LDS_B + q * 112 + kp * 4;
        *LDSP(unsigned, dst + 0 * 5488) = __builtin_amdgcn_perm(vb[0], va[0], 0x05040100u);
        *LDSP(unsigned, dst + 1 * 5488) = __builtin_amdgcn_perm(vb[0], va[0], 0x07060302u);
        *LDSP(unsigned, dst + 2 * 5488) = __builtin_amdgcn_perm(vb[1], va[1], 0x05040100u);
        *LDSP(unsigned, dst + 3 * 5488) = __builtin_amdgcn_perm(vb[1], va[1], 0x07060302u);
      }
    }
    if (t < 49) {
      const u32x2 va = *LDSP(u32x2, sb + LDS_RPB + i48 * 8);
      const unsigned dst = sb + LDS_B + t * 112 + 96;
      *LDSP(u32x2, dst + 0 * 5488) = u32x2{va[0] << 16, 0xff800000u};
      *LDSP(u32x2, dst + 1 * 5488) = u32x2{va[0] & 0xffff0000u, 0xff800000u};
      *LDSP(u32x2, dst + 2 * 5488) = u32x2{va[1] << 16, 0xff800000u};
      *LDSP(u32x2, dst + 3 * 5488) = u32x2{va[1] & 0xffff0000u, 0xff800000u};
    }
    STAMP(11);
    if (P == 0) WAIT_VM(9); else WAIT_VM(6);
    WAIT_LGKM0();
    FENCE();
    __builtin_amdgcn_s_barrier();
    FENCE();
    STAMP(12);
    if (P == 0) WAIT_VM(3); else WAIT_VM(2);
    flag_set(sb + LDS_FLAG + (wv * 4 + 0) * 4);
    flag_wait(sb + LDS_FLAG + ((wv ^ 1) * 4 + 0) * 4);
  }
  STAMP(4);

  bf16x8 xf[2][6];
  {
#pragma unroll
    for (int u = 0; u < 2; ++u) {
      int tok = 32 * u + l31;
      tok = tok > 48 ? 48 : tok;
      const unsigned base = xreg + tok * 192;
      const unsigned sw = (unsigned)(h ^ ((tok >> 2) & 3));
      const unsigned aE = base + sw * 16, aO = base + (sw ^ 2) * 16;
#pragma unroll
      for (int s = 0; s < 6; ++s) xf[u][s] = ldsr128(((s & 1) ? aO : aE) + (s >> 1) * 64);
    }
  }
  const int li = lane & 15, tq = li >> 2, tp = li & 3, g1 = (lane >> 4) & 1;
  const unsigned wbase = sb + LDS_W + P * W_IMG + (8 * h + tq) * 64 + g1 * 32 + tp * 8;

  bf16x8 qf01[2][2], qf2[2][2];
  bf16x8 kf01[2][2], kf22[2];
  bf16x8 vf[2][4];
  const float qscale = 0.20412414523193154f;
  const f32x16 zero16 = {0, 0, 0, 0, 0, 0, 0, 0, 0, 0, 0, 0, 0, 0, 0, 0};
  {
#pragma unroll
    for (int T = 0; T < 3; ++T) {
      f32x16 a0 = zero16, a1 = zero16;
#pragma unroll
      for (int s = 0; s < 6; ++s) {
        const bf16x8 wf = tr2(wbase + T * 6144 + s * 1024, wbase + T * 6144 + s * 1024 + 256);
        a0 = mfma(wf, xf[0][s], a0);
        a1 = mfma(wf, xf[1][s], a1);
      }
      if (T == 0) {
#pragma unroll
        for (int r = 0; r < 16; ++r) { a0[r] *= qscale; a1[r] *= qscale; }
        qf01[0][0] = pack8(a0, 0); qf01[0][1] = pack8(a0, 8);
        qf01[1][0] = pack8(a1, 0); qf01[1][1] = pack8(a1, 8);
      } else if (T == 1) {
        kf01[0][0] = pack8(a0, 0); kf01[0][1] = pack8(a0, 8);
        kf01[1][0] = pack8(a1, 0); kf01[1][1] = pack8(a1, 8);
      } else {
#pragma unroll
        for (int r = 0; r < 8; ++r) { a0[r] *= qscale; a1[r] *= qscale; }
        qf2[0][0] = mk8(cvtpk(a0[0], a0[1]), cvtpk(a0[2], a0[3]), 0u, 0u);
        qf2[0][1] = mk8(0u, 0u, cvtpk(a0[4], a0[5]), cvtpk(a0[6], a0[7]));
        qf2[1][0] = mk8(cvtpk(a1[0], a1[1]), cvtpk(a1[2], a1[3]), 0u, 0u);
        qf2[1][1] = mk8(0u, 0u, cvtpk(a1[4], a1[5]), cvtpk(a1[6], a1[7]));
        kf22[0] = pack8(a0, 8);
        kf22[1] = pack8(a1, 8);
      }
    }
    f32x16 cone;
    {
      const float cv = l31 >= 24 ? 1.0f : 0.0f;
#pragma unroll
      for (int r = 0; r < 16; ++r) cone[r] = cv;
    }
#pragma unroll
    for (int hd = 0; hd < 2; ++hd) {
      f32x16 a0 = cone, a1 = cone;
#pragma unroll
      for (int s = 0; s < 6; ++s) {
        const bf16x8 wf = tr2(wbase + (3 + hd) * 6144 + s * 1024, wbase + (3 + hd) * 6144 + s * 1024 + 256);
        a0 = mfma(xf[0][s], wf, a0);
        a1 = mfma(xf[1][s], wf, a1);
      }
      vf[hd][0] = pack8(a0, 0);
      vf[hd][1] = pack8(a0, 8);
      vf[hd][2] = pack8(a1, 0);
      vf[hd][3] = mk8(cvtpk(a1[8], a1[9]), 0u, 0u, 0u);
    }
  }
  STAMP(5);
  WAIT_VM(0);
  if (late) {
    flag_set(sb + LDS_FLAG + (wv * 4 + 1) * 4);
    flag_wait(sb + LDS_FLAG + ((wv ^ 1) * 4 + 1) * 4);
  } else {
    FENCE();
    __builtin_amdgcn_s_barrier();
    FENCE();
  }
  STAMP(6);

  bf16x8 id0, id1;
  {
    const int d0 = l31 - 8 * h, d1 = l31 - 16 - 8 * h;
    unsigned a[4], c[4];
#pragma unroll
    for (int k = 0; k < 4; ++k) {
      a[k] = (d0 == 2 * k ? 0x3f80u : 0u) | (d0 == 2 * k + 1 ? 0x3f800000u : 0u);
      c[k] = (d1 == 2 * k ? 0x3f80u : 0u) | (d1 == 2 * k + 1 ? 0x3f800000u : 0u);
    }
    id0 = mk8(a[0], a[1], a[2], a[3]);
    id1 = mk8(c[0], c[1], c[2], c[3]);
  }
  const int q1 = 32 + l31 > 48 ? 48 : 32 + l31;
  const unsigned mbase = sb + LDS_M + mdelta + wi * 4802;
  const unsigned bbase = sb + LDS_B + (2 * P) * 5488;
  const unsigned exb = xreg + lane * 16;

  MaskRegs mr;
  f32x16 m0, m1;
  unsigned ogA[6], ogB[6];
  mask_issue(mr, mbase + l31 * 98 + 8 * h, mbase + l31 * 98);
  Bias bi0 = bias_load(bbase + l31 * 112, h);
  Bias bi1 = bias_load(bbase + 5488 + l31 * 112, h);
  mask_wait(mr, m0, m1);
  SB();
  SU su0 = s_unit(kf01[0][0], kf01[1][0], kf22[0], kf22[1], qf01[0][0], qf2[0][0], m0, m1, id0, id1, bi0);
  SB();
  SU su1 = s_unit(kf01[0][1], kf01[1][1], kf22[0], kf22[1], qf01[0][1], qf2[0][1], m0, m1, id0, id1, bi1);
  PU pu0 = softmax_unit(su0);
  Bias bi2 = bias_load(bbase + q1 * 112, h);
  SB();
  mask_issue(mr, mbase + q1 * 98 + 8 * h, mbase + q1 * 98);
  f32x16 o0 = pv_unit(vf[0], pu0);
  Bias bi3 = bias_load(bbase + 5488 + q1 * 112, h);
  mask_wait(mr, m0, m1);
  SB();
  SU su2 = s_unit(kf01[0][0], kf01[1][0], kf22[0], kf22[1], qf01[1][0], qf2[1][0], m0, m1, id0, id1, bi2);
  PU pu1 = softmax_unit(su1);
  norm_unit(o0, ogA);
  SB();
  f32x16 o1 = pv_unit(vf[1], pu1);
  SU su3 = s_unit(kf01[0][1], kf01[1][1], kf22[0], kf22[1], qf01[1][1], qf2[1][1], m0, m1, id0, id1, bi3);
  PU pu2 = softmax_unit(su2);
  norm_unit(o1, ogB);
  *LDSP(bf16x8, exb + P * 3072) = mk8(ogA[0], ogA[1], ogA[2], ogA[3]);
  *LDSP(bf16x8, exb + P * 3072 + 1024) = mk8(ogA[4], ogA[5], ogB[0], ogB[1]);
  *LDSP(bf16x8, exb + P * 3072 + 2048) = mk8(ogB[2], ogB[3], ogB[4], ogB[5]);
  SB();
  WAIT_LGKM0();
  flag_set(sb + LDS_FLAG + (wv * 4 + 2) * 4);
  SB();
  f32x16 o2 = pv_unit(vf[0], pu2);
  PU pu3 = softmax_unit(su3);
  norm_unit(o2, ogA);
  SB();
  f32x16 o3 = pv_unit(vf[1], pu3);
  norm_unit(o3, ogB);
  const bf16x8 t1f0 = mk8(ogA[0], ogA[1], ogA[2], ogA[3]);
  const bf16x8 t1f1 = mk8(ogA[4], ogA[5], ogB[0], ogB[1]);
  const bf16x8 t1f2 = mk8(ogB[2], ogB[3], ogB[4], ogB[5]);
  STAMP(7);

  const unsigned wob = sb + LDS_WO + (4 * h + tq) * 192 + (16 * g1 + 8 * (tp & 1) + 4 * (tp >> 1)) * 2;
  f32x16 acc[3] = {zero16, zero16, zero16};
  if (P == 0) {
    *LDSP(bf16x8, exb + 6144) = t1f0;
    *LDSP(bf16x8, exb + 6144 + 1024) = t1f1;
    *LDSP(bf16x8, exb + 6144 + 2048) = t1f2;
    WAIT_LGKM0();
    flag_set(sb + LDS_FLAG + (wv * 4 + 3) * 4);
    flag_wait(sb + LDS_FLAG + ((wv ^ 1) * 4 + 2) * 4);
#pragma unroll
    for (int s = 0; s < 6; ++s) {
      const bf16x8 bfr = ldsr128(exb + s * 1024);
#pragma unroll
      for (int T = 0; T < 3; ++T)
        acc[T] = mfma(tr2(wob + (16 * s) * 192 + 64 * T, wob + (16 * s + 8) * 192 + 64 * T), bfr, acc[T]);
    }
  } else {
#pragma unroll
    for (int s = 3; s < 6; ++s) {
      const bf16x8 bfr = s == 3 ? t1f0 : (s == 4 ? t1f1 : t1f2);
#pragma unroll
      for (int T = 0; T < 3; ++T)
        acc[T] = mfma(tr2(wob + (16 * s) * 192 + 64 * T, wob + (16 * s + 8) * 192 + 64 * T), bfr, acc[T]);
    }
    flag_wait(sb + LDS_FLAG + ((wv ^ 1) * 4 + 3) * 4);
#pragma unroll
    for (int s = 0; s < 3; ++s) {
      const bf16x8 bfr = ldsr128(exb + 6144 + s * 1024);
#pragma unroll
      for (int T = 0; T < 3; ++T)
        acc[T] = mfma(tr2(wob + (16 * s) * 192 + 64 * T, wob + (16 * s + 8) * 192 + 64 * T), bfr, acc[T]);
    }
  }
  STAMP(8);
  {
    int tok = 32 * P + l31;
    const bool tv = tok < 49;
    tok = tv ? tok : 48;
    const unsigned g = (unsigned)((tok >> 2) & 3);
    const unsigned base = xreg + tok * 192;
    const unsigned a0 = base + ((h ^ g) << 4), a1 = base + (((2 + h) ^ g) << 4);
    if (tv) {
#pragma unroll
      for (int T = 0; T < 3; ++T) {
        *LDSP(bf16x8, a0 + 64 * T) = pack8(acc[T], 0);
        *LDSP(bf16x8, a1 + 64 * T) = pack8(acc[T], 8);
      }
    }
    const unsigned rb = xreg + P * 6144 + lane * 16;
    const unsigned lpb = sb + LDS_LP + P * 1536 + lane * 4;
#pragma unroll
    for (int jj = 0; jj < 6; ++jj) {
      if (jj < 3 || P == 0 || (jj == 3 && lane < 12)) {
        const unsigned lp = *LDSP(unsigned, lpb + jj * 256);
        unsigned o = base0 + (lp & ~15u);
        if (wrap) o -= ((lp & 1u) ? wrapY : 0u) + ((lp & 2u) ? wrapX : 0u);
        const bf16x8 vv = ldsr128(rb + jj * 1024);
        asm volatile("global_store_dwordx4 %0, %1, %2 sc1\n\ts_nop 1" ::"v"(o), "v"(vv), "s"(out) : "memory");
      }
    }
  }
}

extern "C" void kernel_launch(void* const* d_in, const int* in_sizes, int n_in, void* d_out, int out_size, void* d_ws,
                              size_t ws_size, hipStream_t stream) {
  swin_attn_kernel<<<256, 512, 0, stream>>>((const unsigned short*)d_in[0], (const unsigned short*)d_in[1],
                                            (const unsigned short*)d_in[2], (const unsigned short*)d_in[3],
                                            (const int*)d_in[4], (const unsigned short*)d_in[5], (unsigned short*)d_out
  );
}
```

```cpp
#include <hip/hip_runtime.h>
#include <stdint.h>

__global__ __launch_bounds__(512, 2) void swin_attn_kernel(
    const unsigned short* __restrict__ x, const unsigned short* __restrict__ wqkv, const unsigned short* __restrict__ wo,
    const unsigned short* __restrict__ rpb, const int* __restrict__ rp_idx, const unsigned short* __restrict__ mask,
    unsigned short* __restrict__ out) {
  __shared__ __attribute__((aligned(16))) char smem[156944];
#if defined(__HIP_DEVICE_COMPILE__)
  const unsigned sb = (unsigned)(uintptr_t)(__attribute__((address_space(3))) char*)smem;
  asm volatile(
      "s_load_dwordx8 s[8:15], s[4:5], 0\n"
      "s_load_dwordx4 s[16:19], s[4:5], 32\n"
      "s_load_dwordx2 s[20:21], s[4:5], 48\n"
      "v_lshrrev_b32 v8, 6, v0\n"
      "v_and_b32 v1, 63, v0\n"
      "v_readfirstlane_b32 s24, v8\n"
      "v_and_b32 v2, 31, v0\n"
      "v_bfe_u32 v3, v0, 5, 1\n"
      "s_mov_b32 s92, 0x400000\n"
      "s_lshr_b32 s40, s6, 3\n"
      "s_and_b32 s41, s6, 7\n"
      "s_and_b32 s25, s24, 1\n"
      "s_lshr_b32 s26, s24, 1\n"
      "s_lshr_b32 s27, s24, 2\n"
      "s_xor_b32 s35, s25, 1\n"
      "s_lshl_b32 s41, s41, 2\n"
      "s_add_u32 s41, s41, s26\n"
      "s_mul_i32 s28, s40, 301056\n"
      "s_mul_i32 s42, s41, 1344\n"
      "s_add_u32 s28, s28, s42\n"
      "s_add_u32 s28, s28, 129600\n"
      "s_cmp_eq_u32 s40, 31\n"
      "s_cselect_b32 s29, 9633792, 0\n"
      "s_cmp_eq_u32 s41, 31\n"
      "s_cselect_b32 s30, 43008, 0\n"
      "s_or_b32 s31, s29, s30\n"
      "s_mul_i32 s32, s26, 9408\n"
      "s_add_u32 s32, s32, s7\n"
      "s_add_u32 s32, s32, 73728\n"
      "s_mul_i32 s42, s6, 19208\n"
      "s_and_b32 s33, s42, 15\n"
      "s_lshr_b32 s34, s42, 4\n"
      "s_cmp_lg_u32 s25, 0\n"
      "s_cselect_b64 s[46:47], 0xfff, -1\n"
      "s_mul_i32 s43, s25, 384\n"
      "v_add_u32 v9, s43, v1\n"
      "v_mul_u32_u24 v4, 781, v9\n"
      "v_mul_u32_u24 v5, 1366, v9\n"
      "v_lshrrev_b32 v4, 16, v4\n"
      "v_bfe_u32 v5, v5, 16, 2\n"
      "v_xor_b32 v5, v9, v5\n"
      "v_mul_u32_u24 v10, 41664, v4\n"
      "v_lshl_add_u32 v10, v5, 4, v10\n"
      "v_mul_u32_u24 v5, 84, v4\n"
      "v_sub_u32 v5, v9, v5\n"
      "v_cmp_lt_u32 vcc, 3, v4\n"
      "v_cmp_lt_u32 s[58:59], 47, v5\n"
      "s_nop 0\n"
      "v_cndmask_b32 v4, 0, 1, vcc\n"
      "v_cndmask_b32 v5, 0, 2, s[58:59]\n"
      "v_or_b32 v16, v4, v5\n"
      "v_add_u32 v8, 64, v9\n"
      "v_mul_u32_u24 v4, 781, v8\n"
      "v_mul_u32_u24 v5, 1366, v8\n"
      "v_lshrrev_b32 v4, 16, v4\n"
      "v_bfe_u32 v5, v5, 16, 2\n"
      "v_xor_b32 v5, v8, v5\n"
      "v_mul_u32_u24 v11, 41664, v4\n"
      "v_lshl_add_u32 v11, v5, 4, v11\n"
      "v_mul_u32_u24 v5, 84, v4\n"
      "v_sub_u32 v5, v8, v5\n"
      "v_cmp_lt_u32 vcc, 3, v4\n"
      "v_cmp_lt_u32 s[58:59], 47, v5\n"
      "s_nop 0\n"
      "v_cndmask_b32 v4, 0, 1, vcc\n"
      "v_cndmask_b32 v5, 0, 2, s[58:59]\n"
      "v_or_b32 v17, v4, v5\n"
      "v_add_u32 v8, 128, v9\n"
      "v_mul_u32_u24 v4, 781, v8\n"
      "v_mul_u32_u24 v5, 1366, v8\n"
      "v_lshrrev_b32 v4, 16, v4\n"
      "v_bfe_u32 v5, v5, 16, 2\n"
      "v_xor_b32 v5, v8, v5\n"
      "v_mul_u32_u24 v12, 41664, v4\n"
      "v_lshl_add_u32 v12, v5, 4, v12\n"
      "v_mul_u32_u24 v5, 84, v4\n"
      "v_sub_u32 v5, v8, v5\n"
      "v_cmp_lt_u32 vcc, 3, v4\n"
      "v_cmp_lt_u32 s[58:59], 47, v5\n"
      "s_nop 0\n"
      "v_cndmask_b32 v4, 0, 1, vcc\n"
      "v_cndmask_b32 v5, 0, 2, s[58:59]\n"
      "v_or_b32 v18, v4, v5\n"
      "v_add_u32 v8, 192, v9\n"
      "v_mul_u32_u24 v4, 781, v8\n"
      "v_mul_u32_u24 v5, 1366, v8\n"
      "v_lshrrev_b32 v4, 16, v4\n"
      "v_bfe_u32 v5, v5, 16, 2\n"
      "v_xor_b32 v5, v8, v5\n"
      "v_mul_u32_u24 v13, 41664, v4\n"
      "v_lshl_add_u32 v13, v5, 4, v13\n"
      "v_mul_u32_u24 v5, 84, v4\n"
      "v_sub_u32 v5, v8, v5\n"
      "v_cmp_lt_u32 vcc, 3, v4\n"
      "v_cmp_lt_u32 s[58:59], 47, v5\n"
      "s_nop 0\n"
      "v_cndmask_b32 v4, 0, 1, vcc\n"
      "v_cndmask_b32 v5, 0, 2, s[58:59]\n"
      "v_or_b32 v19, v4, v5\n"
      "v_add_u32 v8, 256, v9\n"
      "v_mul_u32_u24 v4, 781, v8\n"
      "v_mul_u32_u24 v5, 1366, v8\n"
      "v_lshrrev_b32 v4, 16, v4\n"
      "v_bfe_u32 v5, v5, 16, 2\n"
      "v_xor_b32 v5, v8, v5\n"
      "v_mul_u32_u24 v14, 41664, v4\n"
      "v_lshl_add_u32 v14, v5, 4, v14\n"
      "v_mul_u32_u24 v5, 84, v4\n"
      "v_sub_u32 v5, v8, v5\n"
      "v_cmp_lt_u32 vcc, 3, v4\n"
      "v_cmp_lt_u32 s[58:59], 47, v5\n"
      "s_nop 0\n"
      "v_cndmask_b32 v4, 0, 1, vcc\n"
      "v_cndmask_b32 v5, 0, 2, s[58:59]\n"
      "v_or_b32 v20, v4, v5\n"
      "v_add_u32 v8, 320, v9\n"
      "v_mul_u32_u24 v4, 781, v8\n"
      "v_mul_u32_u24 v5, 1366, v8\n"
      "v_lshrrev_b32 v4, 16, v4\n"
      "v_bfe_u32 v5, v5, 16, 2\n"
      "v_xor_b32 v5, v8, v5\n"
      "v_mul_u32_u24 v15, 41664, v4\n"
      "v_lshl_add_u32 v15, v5, 4, v15\n"
      "v_mul_u32_u24 v5, 84, v4\n"
      "v_sub_u32 v5, v8, v5\n"
      "v_cmp_lt_u32 vcc, 3, v4\n"
      "v_cmp_lt_u32 s[58:59], 47, v5\n"
      "s_nop 0\n"
      "v_cndmask_b32 v4, 0, 1, vcc\n"
      "v_cndmask_b32 v5, 0, 2, s[58:59]\n"
      "v_or_b32 v21, v4, v5\n"
      "s_cmp_lg_u32 s27, 0\n"
      "s_cbranch_scc1 Lissue1_%=\n"
      "v_add_u32 v10, s28, v10\n"
      "v_add_u32 v11, s28, v11\n"
      "v_add_u32 v12, s28, v12\n"
      "v_add_u32 v13, s28, v13\n"
      "v_add_u32 v14, s28, v14\n"
      "v_add_u32 v15, s28, v15\n"
      "s_cmp_eq_u32 s31, 0\n"
      "s_cbranch_scc1 Xw3_%=\n"
      "v_bfe_i32 v4, v16, 0, 1\n"
      "v_bfe_i32 v5, v16, 1, 1\n"
      "v_and_b32 v4, s29, v4\n"
      "v_and_b32 v5, s30, v5\n"
      "v_sub_u32 v10, v10, v4\n"
      "v_sub_u32 v10, v10, v5\n"
      "v_bfe_i32 v4, v17, 0, 1\n"
      "v_bfe_i32 v5, v17, 1, 1\n"
      "v_and_b32 v4, s29, v4\n"
      "v_and_b32 v5, s30, v5\n"
      "v_sub_u32 v11, v11, v4\n"
      "v_sub_u32 v11, v11, v5\n"
      "v_bfe_i32 v4, v18, 0, 1\n"
      "v_bfe_i32 v5, v18, 1, 1\n"
      "v_and_b32 v4, s29, v4\n"
      "v_and_b32 v5, s30, v5\n"
      "v_sub_u32 v12, v12, v4\n"
      "v_sub_u32 v12, v12, v5\n"
      "v_bfe_i32 v4, v19, 0, 1\n"
      "v_bfe_i32 v5, v19, 1, 1\n"
      "v_and_b32 v4, s29, v4\n"
      "v_and_b32 v5, s30, v5\n"
      "v_sub_u32 v13, v13, v4\n"
      "v_sub_u32 v13, v13, v5\n"
      "v_bfe_i32 v4, v20, 0, 1\n"
      "v_bfe_i32 v5, v20, 1, 1\n"
      "v_and_b32 v4, s29, v4\n"
      "v_and_b32 v5, s30, v5\n"
      "v_sub_u32 v14, v14, v4\n"
      "v_sub_u32 v14, v14, v5\n"
      "v_bfe_i32 v4, v21, 0, 1\n"
      "v_bfe_i32 v5, v21, 1, 1\n"
      "v_and_b32 v4, s29, v4\n"
      "v_and_b32 v5, s30, v5\n"
      "v_sub_u32 v15, v15, v4\n"
      "v_sub_u32 v15, v15, v5\n"
      "Xw3_%=:\n"
      "s_lshl_b32 s40, s35, 1\n"
      "s_add_u32 s40, s40, s26\n"
      "s_lshr_b32 s41, s6, 3\n"
      "s_mul_i32 s41, s41, 7\n"
      "s_add_u32 s40, s40, s41\n"
      "s_cmp_ge_u32 s40, 216\n"
      "s_cselect_b32 s41, 216, 0\n"
      "s_sub_u32 s40, s40, s41\n"
      "s_cmp_ge_u32 s40, 108\n"
      "s_cselect_b32 s41, 108, 0\n"
      "s_sub_u32 s40, s40, s41\n"
      "s_cmp_ge_u32 s40, 54\n"
      "s_cselect_b32 s41, 54, 0\n"
      "s_sub_u32 s40, s40, s41\n"
      "s_lshl_b32 s36, s40, 10\n"
      "s_mov_b32 s37, 55296\n"
      "v_lshlrev_b32 v22, 4, v1\n"
      "s_mul_i32 s40, s26, 4802\n"
      "s_add_u32 s40, s40, s33\n"
      "s_lshr_b32 s40, s40, 4\n"
      "s_lshl_b32 s41, s25, 6\n"
      "s_add_u32 s40, s40, s41\n"
      "s_add_u32 s41, s40, s34\n"
      "v_add_u32 v4, s41, v1\n"
      "s_lshl_b32 s40, s40, 4\n"
      "s_add_u32 s40, s40, s7\n"
      "s_add_u32 s44, s40, 111360\n"
      "v_add_u32 v5, 0, v4\n"
      "v_min_u32 v5, 307327, v5\n"
      "v_lshlrev_b32 v37, 4, v5\n"
      "v_add_u32 v5, 128, v4\n"
      "v_min_u32 v5, 307327, v5\n"
      "v_lshlrev_b32 v38, 4, v5\n"
      "v_add_u32 v5, 256, v4\n"
      "v_min_u32 v5, 307327, v5\n"
      "v_lshlrev_b32 v39, 4, v5\n"
      "s_add_u32 s42, s7, 154256\n"
      "v_mov_b32 v253, s42\n"
      "s_lshl_b32 s40, s24, 2\n"
      "s_add_u32 s40, s40, s42\n"
      "v_mov_b32 v255, s40\n"
      "s_xor_b32 s41, s40, 4\n"
      "v_mov_b32 v254, s41\n"
      "v_bfe_u32 v4, v1, 2, 2\n"
      "v_lshl_add_u32 v5, v3, 3, v4\n"
      "v_mul_u32_u24 v5, 576, v5\n"
      "v_bfe_u32 v6, v1, 4, 1\n"
      "v_and_b32 v7, 3, v1\n"
      "s_mul_i32 s40, s25, 96\n"
      "s_add_u32 s40, s40, s7\n"
      "v_add_u32 v5, s40, v5\n"
      "v_lshl_add_u32 v8, v7, 3, v5\n"
      "v_mul_u32_u24 v9, 48, v6\n"
      "v_add_u32 v252, v8, v9\n"
      "v_lshl_add_u32 v9, v6, 5, v8\n"
      "v_add_u32 v250, 384, v9\n"
      "v_lshrrev_b32 v8, 1, v7\n"
      "v_mul_u32_u24 v8, 48, v8\n"
      "v_mul_u32_u24 v9, 192, v6\n"
      "v_add3_u32 v8, v8, v9, v5\n"
      "v_and_b32 v9, 1, v7\n"
      "v_lshl_add_u32 v8, v9, 3, v8\n"
      "v_add_u32 v251, 32, v8\n"
      "v_lshl_add_u32 v5, v3, 2, v4\n"
      "v_mul_u32_u24 v5, 192, v5\n"
      "v_lshl_add_u32 v5, v6, 5, v5\n"
      "v_and_b32 v8, 1, v7\n"
      "v_lshl_add_u32 v5, v8, 4, v5\n"
      "v_lshrrev_b32 v8, 1, v7\n"
      "v_lshl_add_u32 v5, v8, 3, v5\n"
      "s_add_u32 s40, s7, 55296\n"
      "v_add_u32 v244, s40, v5\n"
      "v_add_u32 v4, 32, v2\n"
      "v_min_u32 v245, 48, v4\n"
      "v_mul_u32_u24 v4, 192, v2\n"
      "v_add_u32 v4, s32, v4\n"
      "v_bfe_u32 v5, v2, 2, 2\n"
      "v_xor_b32 v5, v5, v3\n"
      "v_lshl_add_u32 v249, v5, 4, v4\n"
      "v_xor_b32 v5, 2, v5\n"
      "v_lshl_add_u32 v248, v5, 4, v4\n"
      "v_mul_u32_u24 v4, 192, v245\n"
      "v_add_u32 v4, s32, v4\n"
      "v_bfe_u32 v5, v245, 2, 2\n"
      "v_xor_b32 v5, v5, v3\n"
      "v_lshl_add_u32 v247, v5, 4, v4\n"
      "v_xor_b32 v5, 2, v5\n"
      "v_lshl_add_u32 v246, v5, 4, v4\n"
      "s_mov_b32 s48, 0x3fb8aa3b\n"
      "s_waitcnt lgkmcnt(0)\n"
      "s_mul_i32 s40, s25, 6144\n"
      "s_add_u32 m0, s40, s32\n"
      "s_nop 0\n"
      "global_load_lds_dwordx4 v10, s[8:9] nt\n"
      "s_add_u32 m0, m0, 0x400\n"
      "s_nop 0\n"
      "global_load_lds_dwordx4 v11, s[8:9] nt\n"
      "s_add_u32 m0, m0, 0x400\n"
      "s_nop 0\n"
      "global_load_lds_dwordx4 v12, s[8:9] nt\n"
      "s_add_u32 m0, m0, 0x400\n"
      "s_nop 0\n"
      "s_mov_b64 exec, s[46:47]\n"
      "global_load_lds_dwordx4 v13, s[8:9] nt\n"
      "s_mov_b64 exec, -1\n"
      "s_cmp_lg_u32 s25, 0\n"
      "s_cbranch_scc1 Xend4_%=\n"
      "s_add_u32 m0, m0, 0x400\n"
      "s_nop 0\n"
      "global_load_lds_dwordx4 v14, s[8:9] nt\n"
      "s_add_u32 m0, m0, 0x400\n"
      "s_nop 0\n"
      "global_load_lds_dwordx4 v15, s[8:9] nt\n"
      "Xend4_%=:\n"
      "s_barrier\n"
      "s_add_u32 m0, s36, s7\n"
      "v_add_u32 v23, s36, v22\n"
      "global_load_lds_dwordx4 v23, s[10:11]\n"
      "s_add_u32 s36, s36, 4096\n"
      "s_cmp_ge_u32 s36, s37\n"
      "s_cselect_b32 s40, s37, 0\n"
      "s_sub_u32 s36, s36, s40\n"
      "s_add_u32 m0, s36, s7\n"
      "v_add_u32 v24, s36, v22\n"
      "global_load_lds_dwordx4 v24, s[10:11]\n"
      "s_add_u32 s36, s36, 4096\n"
      "s_cmp_ge_u32 s36, s37\n"
      "s_cselect_b32 s40, s37, 0\n"
      "s_sub_u32 s36, s36, s40\n"
      "s_add_u32 m0, s36, s7\n"
      "v_add_u32 v23, s36, v22\n"
      "global_load_lds_dwordx4 v23, s[10:11]\n"
      "s_add_u32 s36, s36, 4096\n"
      "s_cmp_ge_u32 s36, s37\n"
      "s_cselect_b32 s40, s37, 0\n"
      "s_sub_u32 s36, s36, s40\n"
      "s_add_u32 m0, s36, s7\n"
      "v_add_u32 v24, s36, v22\n"
      "global_load_lds_dwordx4 v24, s[10:11]\n"
      "s_add_u32 s36, s36, 4096\n"
      "s_cmp_ge_u32 s36, s37\n"
      "s_cselect_b32 s40, s37, 0\n"
      "s_sub_u32 s36, s36, s40\n"
      "s_add_u32 m0, s36, s7\n"
      "v_add_u32 v23, s36, v22\n"
      "global_load_lds_dwordx4 v23, s[10:11]\n"
      "s_add_u32 s36, s36, 4096\n"
      "s_cmp_ge_u32 s36, s37\n"
      "s_cselect_b32 s40, s37, 0\n"
      "s_sub_u32 s36, s36, s40\n"
      "s_add_u32 m0, s36, s7\n"
      "v_add_u32 v24, s36, v22\n"
      "global_load_lds_dwordx4 v24, s[10:11]\n"
      "s_add_u32 s36, s36, 4096\n"
      "s_cmp_ge_u32 s36, s37\n"
      "s_cselect_b32 s40, s37, 0\n"
      "s_sub_u32 s36, s36, s40\n"
      "s_add_u32 m0, s36, s7\n"
      "v_add_u32 v23, s36, v22\n"
      "global_load_lds_dwordx4 v23, s[10:11]\n"
      "s_add_u32 s36, s36, 4096\n"
      "s_cmp_ge_u32 s36, s37\n"
      "s_cselect_b32 s40, s37, 0\n"
      "s_sub_u32 s36, s36, s40\n"
      "s_add_u32 m0, s36, s7\n"
      "v_add_u32 v24, s36, v22\n"
      "global_load_lds_dwordx4 v24, s[10:11]\n"
      "s_add_u32 s36, s36, 4096\n"
      "s_cmp_ge_u32 s36, s37\n"
      "s_cselect_b32 s40, s37, 0\n"
      "s_sub_u32 s36, s36, s40\n"
      "s_add_u32 m0, s36, s7\n"
      "v_add_u32 v23, s36, v22\n"
      "global_load_lds_dwordx4 v23, s[10:11]\n"
      "s_add_u32 s36, s36, 4096\n"
      "s_cmp_ge_u32 s36, s37\n"
      "s_cselect_b32 s40, s37, 0\n"
      "s_sub_u32 s36, s36, s40\n"
      "s_add_u32 m0, s36, s7\n"
      "v_add_u32 v24, s36, v22\n"
      "global_load_lds_dwordx4 v24, s[10:11]\n"
      "s_add_u32 s36, s36, 4096\n"
      "s_cmp_ge_u32 s36, s37\n"
      "s_cselect_b32 s40, s37, 0\n"
      "s_sub_u32 s36, s36, s40\n"
      "s_add_u32 m0, s36, s7\n"
      "v_add_u32 v23, s36, v22\n"
      "global_load_lds_dwordx4 v23, s[10:11]\n"
      "s_add_u32 s36, s36, 4096\n"
      "s_cmp_ge_u32 s36, s37\n"
      "s_cselect_b32 s40, s37, 0\n"
      "s_sub_u32 s36, s36, s40\n"
      "s_add_u32 m0, s36, s7\n"
      "v_add_u32 v24, s36, v22\n"
      "global_load_lds_dwordx4 v24, s[10:11]\n"
      "s_add_u32 s36, s36, 4096\n"
      "s_cmp_ge_u32 s36, s37\n"
      "s_cselect_b32 s40, s37, 0\n"
      "s_sub_u32 s36, s36, s40\n"
      "s_add_u32 m0, s36, s7\n"
      "v_add_u32 v23, s36, v22\n"
      "global_load_lds_dwordx4 v23, s[10:11]\n"
      "s_add_u32 s36, s36, 4096\n"
      "s_cmp_ge_u32 s36, s37\n"
      "s_cselect_b32 s40, s37, 0\n"
      "s_sub_u32 s36, s36, s40\n"
      "s_cmp_eq_u32 s25, 0\n"
      "s_cbranch_scc1 Wend5_%=\n"
      "s_add_u32 m0, s36, s7\n"
      "v_add_u32 v24, s36, v22\n"
      "global_load_lds_dwordx4 v24, s[10:11]\n"
      "Wend5_%=:\n"
      "s_mov_b32 m0, s44\n"
      "s_nop 0\n"
      "global_load_lds_dwordx4 v37, s[18:19] nt\n"
      "s_add_u32 m0, m0, 0x800\n"
      "s_nop 0\n"
      "global_load_lds_dwordx4 v38, s[18:19] nt\n"
      "s_add_u32 m0, m0, 0x800\n"
      "s_cmp_lg_u32 s25, 0\n"
      "s_cbranch_scc1 Mend6_%=\n"
      "global_load_lds_dwordx4 v39, s[18:19] nt\n"
      "Mend6_%=:\n"
      "s_barrier\n"
      "s_branch Pre2_%=\n"
      "Lissue1_%=:\n"
      "s_sub_u32 s50, s24, 4\n"
      "s_lshr_b32 s49, s50, 1\n"
      "v_and_b32 v22, 0xff, v0\n"
      "v_add_u32 v4, 0, v1\n"
      "v_min_u32 v4, 168, v4\n"
      "v_lshlrev_b32 v23, 3, v4\n"
      "v_add_u32 v4, 64, v1\n"
      "v_min_u32 v4, 168, v4\n"
      "v_lshlrev_b32 v24, 3, v4\n"
      "v_add_u32 v4, 128, v1\n"
      "v_min_u32 v4, 168, v4\n"
      "v_lshlrev_b32 v25, 3, v4\n"
      "v_add_u32 v4, 0, v22\n"
      "v_mul_u32_u24 v5, 2731, v4\n"
      "v_lshrrev_b32 v5, 16, v5\n"
      "v_lshlrev_b32 v6, 3, v4\n"
      "v_lshl_add_u32 v26, v5, 2, v6\n"
      "v_lshlrev_b32 v6, 2, v4\n"
      "v_lshl_add_u32 v32, v5, 4, v6\n"
      "v_add_u32 v4, 256, v22\n"
      "v_mul_u32_u24 v5, 2731, v4\n"
      "v_lshrrev_b32 v5, 16, v5\n"
      "v_lshlrev_b32 v6, 3, v4\n"
      "v_lshl_add_u32 v27, v5, 2, v6\n"
      "v_lshlrev_b32 v6, 2, v4\n"
      "v_lshl_add_u32 v33, v5, 4, v6\n"
      "v_add_u32 v4, 512, v22\n"
      "v_mul_u32_u24 v5, 2731, v4\n"
      "v_lshrrev_b32 v5, 16, v5\n"
      "v_lshlrev_b32 v6, 3, v4\n"
      "v_lshl_add_u32 v28, v5, 2, v6\n"
      "v_lshlrev_b32 v6, 2, v4\n"
      "v_lshl_add_u32 v34, v5, 4, v6\n"
      "v_add_u32 v4, 768, v22\n"
      "v_mul_u32_u24 v5, 2731, v4\n"
      "v_lshrrev_b32 v5, 16, v5\n"
      "v_lshlrev_b32 v6, 3, v4\n"
      "v_lshl_add_u32 v29, v5, 2, v6\n"
      "v_lshlrev_b32 v6, 2, v4\n"
      "v_lshl_add_u32 v35, v5, 4, v6\n"
      "v_add_u32 v4, 1024, v22\n"
      "v_min_u32 v4, 1175, v4\n"
      "v_mul_u32_u24 v5, 2731, v4\n"
      "v_lshrrev_b32 v5, 16, v5\n"
      "v_lshlrev_b32 v6, 3, v4\n"
      "v_lshl_add_u32 v30, v5, 2, v6\n"
      "v_lshlrev_b32 v6, 2, v4\n"
      "v_lshl_add_u32 v36, v5, 4, v6\n"
      "v_min_u32 v4, 48, v22\n"
      "v_mul_u32_u24 v31, 196, v4\n"
      "v_mul_u32_u24 v51, 112, v4\n"
      "s_waitcnt lgkmcnt(0)\n"
      "global_load_dwordx2 v[52:53], v23, s[14:15]\n"
      "global_load_dwordx2 v[54:55], v24, s[14:15]\n"
      "global_load_dwordx2 v[56:57], v25, s[14:15]\n"
      "global_load_dwordx2 v[58:59], v26, s[16:17]\n"
      "global_load_dwordx2 v[60:61], v27, s[16:17]\n"
      "global_load_dwordx2 v[62:63], v28, s[16:17]\n"
      "global_load_dwordx2 v[64:65], v29, s[16:17]\n"
      "global_load_dwordx2 v[66:67], v30, s[16:17]\n"
      "global_load_dword v68, v31, s[16:17] offset:192\n"
      "s_barrier\n"
      "s_add_u32 s42, s7, 130944\n"
      "v_add_u32 v32, s42, v32\n"
      "v_add_u32 v33, s42, v33\n"
      "v_add_u32 v34, s42, v34\n"
      "v_add_u32 v35, s42, v35\n"
      "v_add_u32 v36, s42, v36\n"
      "v_add_u32 v51, s42, v51\n"
      "s_add_u32 s42, s7, 152896\n"
      "v_add_u32 v23, s42, v23\n"
      "v_add_u32 v24, s42, v24\n"
      "v_add_u32 v25, s42, v25\n"
      "s_mul_i32 s40, s25, 1536\n"
      "s_add_u32 s40, s40, s7\n"
      "s_add_u32 s40, s40, 154384\n"
      "v_lshlrev_b32 v7, 2, v1\n"
      "v_add_u32 v7, s40, v7\n"
      "s_cmp_lg_u32 s49, 0\n"
      "s_cselect_b64 exec, 0, -1\n"
      "v_or_b32 v4, v10, v16\n"
      "ds_write_b32 v7, v4 offset:0\n"
      "v_or_b32 v4, v11, v17\n"
      "ds_write_b32 v7, v4 offset:256\n"
      "v_or_b32 v4, v12, v18\n"
      "ds_write_b32 v7, v4 offset:512\n"
      "v_or_b32 v4, v13, v19\n"
      "ds_write_b32 v7, v4 offset:768\n"
      "s_cmp_lg_u32 s50, 0\n"
      "s_cselect_b64 exec, 0, -1\n"
      "v_or_b32 v4, v14, v20\n"
      "ds_write_b32 v7, v4 offset:1024\n"
      "v_or_b32 v4, v15, v21\n"
      "ds_write_b32 v7, v4 offset:1280\n"
      "s_mov_b64 exec, -1\n"
      "v_add_u32 v10, s28, v10\n"
      "v_add_u32 v11, s28, v11\n"
      "v_add_u32 v12, s28, v12\n"
      "v_add_u32 v13, s28, v13\n"
      "v_add_u32 v14, s28, v14\n"
      "v_add_u32 v15, s28, v15\n"
      "s_cmp_eq_u32 s31, 0\n"
      "s_cbranch_scc1 Xw7_%=\n"
      "v_bfe_i32 v4, v16, 0, 1\n"
      "v_bfe_i32 v5, v16, 1, 1\n"
      "v_and_b32 v4, s29, v4\n"
      "v_and_b32 v5, s30, v5\n"
      "v_sub_u32 v10, v10, v4\n"
      "v_sub_u32 v10, v10, v5\n"
      "v_bfe_i32 v4, v17, 0, 1\n"
      "v_bfe_i32 v5, v17, 1, 1\n"
      "v_and_b32 v4, s29, v4\n"
      "v_and_b32 v5, s30, v5\n"
      "v_sub_u32 v11, v11, v4\n"
      "v_sub_u32 v11, v11, v5\n"
      "v_bfe_i32 v4, v18, 0, 1\n"
      "v_bfe_i32 v5, v18, 1, 1\n"
      "v_and_b32 v4, s29, v4\n"
      "v_and_b32 v5, s30, v5\n"
      "v_sub_u32 v12, v12, v4\n"
      "v_sub_u32 v12, v12, v5\n"
      "v_bfe_i32 v4, v19, 0, 1\n"
      "v_bfe_i32 v5, v19, 1, 1\n"
      "v_and_b32 v4, s29, v4\n"
      "v_and_b32 v5, s30, v5\n"
      "v_sub_u32 v13, v13, v4\n"
      "v_sub_u32 v13, v13, v5\n"
      "v_bfe_i32 v4, v20, 0, 1\n"
      "v_bfe_i32 v5, v20, 1, 1\n"
      "v_and_b32 v4, s29, v4\n"
      "v_and_b32 v5, s30, v5\n"
      "v_sub_u32 v14, v14, v4\n"
      "v_sub_u32 v14, v14, v5\n"
      "v_bfe_i32 v4, v21, 0, 1\n"
      "v_bfe_i32 v5, v21, 1, 1\n"
      "v_and_b32 v4, s29, v4\n"
      "v_and_b32 v5, s30, v5\n"
      "v_sub_u32 v15, v15, v4\n"
      "v_sub_u32 v15, v15, v5\n"
      "Xw7_%=:\n"
      "s_mul_i32 s40, s26, 4802\n"
      "s_add_u32 s40, s40, s33\n"
      "s_lshr_b32 s40, s40, 4\n"
      "s_lshl_b32 s41, s25, 6\n"
      "s_add_u32 s40, s40, s41\n"
      "s_add_u32 s41, s40, s34\n"
      "v_add_u32 v4, s41, v1\n"
      "s_lshl_b32 s40, s40, 4\n"
      "s_add_u32 s40, s40, s7\n"
      "s_add_u32 s44, s40, 111360\n"
      "v_add_u32 v5, 0, v4\n"
      "v_min_u32 v5, 307327, v5\n"
      "v_lshlrev_b32 v37, 4, v5\n"
      "v_add_u32 v5, 128, v4\n"
      "v_min_u32 v5, 307327, v5\n"
      "v_lshlrev_b32 v38, 4, v5\n"
      "v_add_u32 v5, 256, v4\n"
      "v_min_u32 v5, 307327, v5\n"
      "v_lshlrev_b32 v39, 4, v5\n"
      "s_lshl_b32 s40, s50, 10\n"
      "v_lshlrev_b32 v46, 4, v1\n"
      "v_add_u32 v46, s40, v46\n"
      "v_add_u32 v47, 4096, v46\n"
      "v_add_u32 v48, 8192, v46\n"
      "v_add_u32 v49, 12288, v46\n"
      "s_sub_u32 s41, 5, s50\n"
      "s_min_u32 s41, s41, 4\n"
      "s_lshl_b32 s41, s41, 10\n"
      "v_add_u32 v50, s41, v49\n"
      "s_add_u32 s42, s7, 154256\n"
      "v_mov_b32 v253, s42\n"
      "s_lshl_b32 s40, s24, 2\n"
      "s_add_u32 s40, s40, s42\n"
      "v_mov_b32 v255, s40\n"
      "s_xor_b32 s41, s40, 4\n"
      "v_mov_b32 v254, s41\n"
      "v_bfe_u32 v4, v1, 2, 2\n"
      "v_lshl_add_u32 v5, v3, 3, v4\n"
      "v_mul_u32_u24 v5, 576, v5\n"
      "v_bfe_u32 v6, v1, 4, 1\n"
      "v_and_b32 v7, 3, v1\n"
      "s_mul_i32 s40, s25, 96\n"
      "s_add_u32 s40, s40, s7\n"
      "v_add_u32 v5, s40, v5\n"
      "v_lshl_add_u32 v8, v7, 3, v5\n"
      "v_mul_u32_u24 v9, 48, v6\n"
      "v_add_u32 v252, v8, v9\n"
      "v_lshl_add_u32 v9, v6, 5, v8\n"
      "v_add_u32 v250, 384, v9\n"
      "v_lshrrev_b32 v8, 1, v7\n"
      "v_mul_u32_u24 v8, 48, v8\n"
      "v_mul_u32_u24 v9, 192, v6\n"
      "v_add3_u32 v8, v8, v9, v5\n"
      "v_and_b32 v9, 1, v7\n"
      "v_lshl_add_u32 v8, v9, 3, v8\n"
      "v_add_u32 v251, 32, v8\n"
      "v_lshl_add_u32 v5, v3, 2, v4\n"
      "v_mul_u32_u24 v5, 192, v5\n"
      "v_lshl_add_u32 v5, v6, 5, v5\n"
      "v_and_b32 v8, 1, v7\n"
      "v_lshl_add_u32 v5, v8, 4, v5\n"
      "v_lshrrev_b32 v8, 1, v7\n"
      "v_lshl_add_u32 v5, v8, 3, v5\n"
      "s_add_u32 s40, s7, 55296\n"
      "v_add_u32 v244, s40, v5\n"
      "v_add_u32 v4, 32, v2\n"
      "v_min_u32 v245, 48, v4\n"
      "v_mul_u32_u24 v4, 192, v2\n"
      "v_add_u32 v4, s32, v4\n"
      "v_bfe_u32 v5, v2, 2, 2\n"
      "v_xor_b32 v5, v5, v3\n"
      "v_lshl_add_u32 v249, v5, 4, v4\n"
      "v_xor_b32 v5, 2, v5\n"
      "v_lshl_add_u32 v248, v5, 4, v4\n"
      "v_mul_u32_u24 v4, 192, v245\n"
      "v_add_u32 v4, s32, v4\n"
      "v_bfe_u32 v5, v245, 2, 2\n"
      "v_xor_b32 v5, v5, v3\n"
      "v_lshl_add_u32 v247, v5, 4, v4\n"
      "v_xor_b32 v5, 2, v5\n"
      "v_lshl_add_u32 v246, v5, 4, v4\n"
      "s_mov_b32 s48, 0x3fb8aa3b\n"
      "v_lshlrev_b32 v4, 2, v1\n"
      "v_add_u32 v4, v4, v253\n"
      "v_mov_b32 v5, 0\n"
      "s_cmp_lg_u32 s50, 0\n"
      "s_cselect_b32 exec_lo, 0, -1\n"
      "s_mov_b32 exec_hi, 0\n"
      "ds_write_b32 v4, v5\n"
      "s_mov_b64 exec, -1\n"
      "v_mov_b32 v91, 0xff800000\n"
      "v_mov_b32 v93, 0xff800000\n"
      "v_mov_b32 v95, 0xff800000\n"
      "v_mov_b32 v97, 0xff800000\n"
      "s_mov_b32 s40, 0x05040100\n"
      "s_mov_b32 s41, 0x07060302\n"
      "s_add_u32 s42, s7, 152896\n"
      "s_waitcnt vmcnt(6)\n"
      "ds_write_b64 v23, v[52:53]\n"
      "ds_write_b64 v24, v[54:55]\n"
      "ds_write_b64 v25, v[56:57]\n"
      "s_waitcnt vmcnt(0)\n"
      "v_lshl_add_u32 v58, v58, 3, s42\n"
      "v_lshl_add_u32 v59, v59, 3, s42\n"
      "v_lshl_add_u32 v60, v60, 3, s42\n"
      "v_lshl_add_u32 v61, v61, 3, s42\n"
      "v_lshl_add_u32 v62, v62, 3, s42\n"
      "v_lshl_add_u32 v63, v63, 3, s42\n"
      "v_lshl_add_u32 v64, v64, 3, s42\n"
      "v_lshl_add_u32 v65, v65, 3, s42\n"
      "v_lshl_add_u32 v66, v66, 3, s42\n"
      "v_lshl_add_u32 v67, v67, 3, s42\n"
      "v_lshl_add_u32 v68, v68, 3, s42\n"
      "s_waitcnt lgkmcnt(0)\n"
      "ds_read_b64 v[70:71], v58\n"
      "ds_read_b64 v[72:73], v59\n"
      "ds_read_b64 v[74:75], v60\n"
      "ds_read_b64 v[76:77], v61\n"
      "ds_read_b64 v[78:79], v62\n"
      "ds_read_b64 v[80:81], v63\n"
      "ds_read_b64 v[82:83], v64\n"
      "ds_read_b64 v[84:85], v65\n"
      "ds_read_b64 v[86:87], v66\n"
      "ds_read_b64 v[88:89], v67\n"
      "ds_read_b64 v[52:53], v68\n"
      "s_waitcnt lgkmcnt(0)\n"
      "v_perm_b32 v100, v72, v70, s40\n"
      "v_perm_b32 v101, v72, v70, s41\n"
      "v_perm_b32 v102, v73, v71, s40\n"
      "v_perm_b32 v103, v73, v71, s41\n"
      "ds_write_b32 v32, v100 offset:0\n"
      "ds_write_b32 v32, v101 offset:5488\n"
      "ds_write_b32 v32, v102 offset:10976\n"
      "ds_write_b32 v32, v103 offset:16464\n"
      "v_perm_b32 v104, v76, v74, s40\n"
      "v_perm_b32 v105, v76, v74, s41\n"
      "v_perm_b32 v106, v77, v75, s40\n"
      "v_perm_b32 v107, v77, v75, s41\n"
      "ds_write_b32 v33, v104 offset:0\n"
      "ds_write_b32 v33, v105 offset:5488\n"
      "ds_write_b32 v33, v106 offset:10976\n"
      "ds_write_b32 v33, v107 offset:16464\n"
      "v_perm_b32 v108, v80, v78, s40\n"
      "v_perm_b32 v109, v80, v78, s41\n"
      "v_perm_b32 v110, v81, v79, s40\n"
      "v_perm_b32 v111, v81, v79, s41\n"
      "ds_write_b32 v34, v108 offset:0\n"
      "ds_write_b32 v34, v109 offset:5488\n"
      "ds_write_b32 v34, v110 offset:10976\n"
      "ds_write_b32 v34, v111 offset:16464\n"
      "v_perm_b32 v112, v84, v82, s40\n"
      "v_perm_b32 v113, v84, v82, s41\n"
      "v_perm_b32 v114, v85, v83, s40\n"
      "v_perm_b32 v115, v85, v83, s41\n"
      "ds_write_b32 v35, v112 offset:0\n"
      "ds_write_b32 v35, v113 offset:5488\n"
      "ds_write_b32 v35, v114 offset:10976\n"
      "ds_write_b32 v35, v115 offset:16464\n"
      "v_perm_b32 v116, v88, v86, s40\n"
      "v_perm_b32 v117, v88, v86, s41\n"
      "v_perm_b32 v118, v89, v87, s40\n"
      "v_perm_b32 v119, v89, v87, s41\n"
      "ds_write_b32 v36, v116 offset:0\n"
      "ds_write_b32 v36, v117 offset:5488\n"
      "ds_write_b32 v36, v118 offset:10976\n"
      "ds_write_b32 v36, v119 offset:16464\n"
      "v_lshlrev_b32 v90, 16, v52\n"
      "v_and_b32 v92, 0xffff0000, v52\n"
      "v_lshlrev_b32 v94, 16, v53\n"
      "v_and_b32 v96, 0xffff0000, v53\n"
      "ds_write_b64 v51, v[90:91] offset:96\n"
      "ds_write_b64 v51, v[92:93] offset:5584\n"
      "ds_write_b64 v51, v[94:95] offset:11072\n"
      "ds_write_b64 v51, v[96:97] offset:16560\n"
      "s_waitcnt lgkmcnt(0)\n"
      "s_barrier\n"
      "s_mul_i32 s40, s25, 6144\n"
      "s_add_u32 m0, s40, s32\n"
      "s_nop 0\n"
      "global_load_lds_dwordx4 v10, s[8:9] nt\n"
      "s_add_u32 m0, m0, 0x400\n"
      "s_nop 0\n"
      "global_load_lds_dwordx4 v11, s[8:9] nt\n"
      "s_add_u32 m0, m0, 0x400\n"
      "s_nop 0\n"
      "global_load_lds_dwordx4 v12, s[8:9] nt\n"
      "s_add_u32 m0, m0, 0x400\n"
      "s_nop 0\n"
      "s_mov_b64 exec, s[46:47]\n"
      "global_load_lds_dwordx4 v13, s[8:9] nt\n"
      "s_mov_b64 exec, -1\n"
      "s_cmp_lg_u32 s25, 0\n"
      "s_cbranch_scc1 Xend8_%=\n"
      "s_add_u32 m0, m0, 0x400\n"
      "s_nop 0\n"
      "global_load_lds_dwordx4 v14, s[8:9] nt\n"
      "s_add_u32 m0, m0, 0x400\n"
      "s_nop 0\n"
      "global_load_lds_dwordx4 v15, s[8:9] nt\n"
      "Xend8_%=:\n"
      "s_lshl_b32 s40, s50, 10\n"
      "s_add_u32 s40, s40, s7\n"
      "s_add_u32 m0, s40, 55296\n"
      "s_sub_u32 s41, 5, s50\n"
      "s_min_u32 s41, s41, 4\n"
      "s_lshl_b32 s41, s41, 10\n"
      "global_load_lds_dwordx4 v46, s[12:13]\n"
      "s_add_u32 m0, m0, 0x1000\n"
      "s_nop 0\n"
      "global_load_lds_dwordx4 v47, s[12:13]\n"
      "s_add_u32 m0, m0, 0x1000\n"
      "s_nop 0\n"
      "global_load_lds_dwordx4 v48, s[12:13]\n"
      "s_add_u32 m0, m0, 0x1000\n"
      "s_nop 0\n"
      "global_load_lds_dwordx4 v49, s[12:13]\n"
      "s_add_u32 m0, m0, s41\n"
      "s_nop 0\n"
      "global_load_lds_dwordx4 v50, s[12:13]\n"
      "s_mov_b32 m0, s44\n"
      "s_nop 0\n"
      "global_load_lds_dwordx4 v37, s[18:19] nt\n"
      "s_add_u32 m0, m0, 0x800\n"
      "s_nop 0\n"
      "global_load_lds_dwordx4 v38, s[18:19] nt\n"
      "s_add_u32 m0, m0, 0x800\n"
      "s_cmp_lg_u32 s25, 0\n"
      "s_cbranch_scc1 Mend9_%=\n"
      "global_load_lds_dwordx4 v39, s[18:19] nt\n"
      "Mend9_%=:\n"
      "Pre2_%=:\n"
      "v_lshlrev_b32 v243, 3, v3\n"
      "v_sub_u32 v243, v2, v243\n"
      "v_and_b32 v241, 1, v243\n"
      "v_lshlrev_b32 v241, 4, v241\n"
      "v_mov_b32 v242, 0x3f80\n"
      "v_lshlrev_b32 v242, v241, v242\n"
      "v_ashrrev_i32 v243, 1, v243\n"
      "v_cmp_eq_u32 vcc, 0, v243\n"
      "s_nop 1\n"
      "v_cndmask_b32 v190, 0, v242, vcc\n"
      "v_cmp_eq_u32 vcc, 1, v243\n"
      "s_nop 1\n"
      "v_cndmask_b32 v191, 0, v242, vcc\n"
      "v_cmp_eq_u32 vcc, 2, v243\n"
      "s_nop 1\n"
      "v_cndmask_b32 v192, 0, v242, vcc\n"
      "v_cmp_eq_u32 vcc, 3, v243\n"
      "s_nop 1\n"
      "v_cndmask_b32 v193, 0, v242, vcc\n"
      "v_cmp_eq_u32 vcc, 8, v243\n"
      "s_nop 1\n"
      "v_cndmask_b32 v186, 0, v242, vcc\n"
      "v_cmp_eq_u32 vcc, 9, v243\n"
      "s_nop 1\n"
      "v_cndmask_b32 v187, 0, v242, vcc\n"
      "v_cmp_eq_u32 vcc, 10, v243\n"
      "s_nop 1\n"
      "v_cndmask_b32 v188, 0, v242, vcc\n"
      "v_cmp_eq_u32 vcc, 11, v243\n"
      "s_nop 1\n"
      "v_cndmask_b32 v189, 0, v242, vcc\n"
      "v_mov_b32 v110, 0\n"
      "v_mov_b32 v111, 0\n"
      "v_mov_b32 v142, 0\n"
      "v_mov_b32 v143, 0\n"
      "v_mov_b32 v167, 0\n"
      "v_mov_b32 v168, 0\n"
      "v_mov_b32 v169, 0\n"
      "v_mov_b32 v182, 0x3f803f80\n"
      "v_mov_b32 v183, 0x3f803f80\n"
      "v_mov_b32 v184, 0x3f803f80\n"
      "v_mov_b32 v185, 0x3f803f80\n"
      "v_mov_b32 v174, 0x3f803f80\n"
      "v_mov_b32 v175, 0x3f803f80\n"
      "v_mov_b32 v176, 0x3f803f80\n"
      "v_mov_b32 v177, 0x3f803f80\n"
      "v_mov_b32 v170, 0x3f803f80\n"
      "v_mov_b32 v171, 0x3f803f80\n"
      "v_mov_b32 v172, 0x3f803f80\n"
      "v_mov_b32 v173, 0x3f803f80\n"
      "v_mov_b32 v166, 0x3f803f80\n"
      "v_mov_b32 v147, 0\n"
      "v_mov_b32 v148, 0\n"
      "v_mov_b32 v149, 0\n"
      "v_mov_b32 v162, 0x3f803f80\n"
      "v_mov_b32 v163, 0x3f803f80\n"
      "v_mov_b32 v164, 0x3f803f80\n"
      "v_mov_b32 v165, 0x3f803f80\n"
      "v_mov_b32 v158, 0x3f803f80\n"
      "v_mov_b32 v159, 0x3f803f80\n"
      "v_mov_b32 v160, 0x3f803f80\n"
      "v_mov_b32 v161, 0x3f803f80\n"
      "v_mov_b32 v150, 0x3f803f80\n"
      "v_mov_b32 v151, 0x3f803f80\n"
      "v_mov_b32 v152, 0x3f803f80\n"
      "v_mov_b32 v153, 0x3f803f80\n"
      "v_mov_b32 v146, 0x3f803f80\n"
      "s_mov_b32 s94, 0xffffff\n"
      "s_mov_b32 s95, 0xffffff\n"
      "v_mov_b32 v179, 0\n"
      "v_mov_b32 v180, 0\n"
      "v_mov_b32 v181, 0\n"
      "v_mov_b32 v155, 0\n"
      "v_mov_b32 v156, 0\n"
      "v_mov_b32 v157, 0\n"
      "s_lshl_b32 s40, s35, 5\n"
      "v_add_u32 v243, s40, v2\n"
      "v_min_u32 v243, 48, v243\n"
      "s_mul_i32 s40, s26, 4802\n"
      "s_add_u32 s40, s40, s33\n"
      "s_add_u32 s40, s40, s7\n"
      "s_add_u32 s40, s40, 111360\n"
      "s_mul_i32 s41, s25, 10976\n"
      "s_add_u32 s41, s41, s7\n"
      "s_add_u32 s41, s41, 130944\n"
      "v_mul_u32_u24 v241, 98, v243\n"
      "v_add_u32 v241, s40, v241\n"
      "v_lshl_add_u32 v239, v3, 3, v241\n"
      "v_mul_u32_u24 v242, 112, v243\n"
      "v_add_u32 v242, s41, v242\n"
      "v_lshl_add_u32 v240, v3, 4, v242\n"
      "v_lshl_add_u32 v242, v3, 2, v242\n"
      "s_cmp_eq_u32 s27, 0\n"
      "s_cbranch_scc1 SyE10_%=\n"
      "s_cmp_lg_u32 s25, 0\n"
      "s_cbranch_scc1 W12_%=a\n"
      "s_waitcnt vmcnt(8)\n"
      "s_branch W12_%=b\n"
      "W12_%=a:\n"
      "s_waitcnt vmcnt(7)\n"
      "W12_%=b:\n"
      "v_mov_b32 v238, 1\n"
      "ds_write_b32 v255, v238 offset:0\n"
      "F13_%=:\n"
      "ds_read_b32 v238, v254 offset:0\n"
      "s_waitcnt lgkmcnt(0)\n"
      "v_readfirstlane_b32 s51, v238\n"
      "s_cmp_lg_u32 s51, 0\n"
      "s_cbranch_scc1 F14_%=\n"
      "s_sleep 1\n"
      "s_sub_u32 s92, s92, 1\n"
      "s_cbranch_scc0 F13_%=\n"
      "F14_%=:\n"
      "s_branch SyG11_%=\n"
      "SyE10_%=:\n"
      "s_cmp_lg_u32 s25, 0\n"
      "s_cbranch_scc1 W15_%=a\n"
      "s_waitcnt vmcnt(3)\n"
      "s_branch W15_%=b\n"
      "W15_%=a:\n"
      "s_waitcnt vmcnt(2)\n"
      "W15_%=b:\n"
      "v_mov_b32 v238, 1\n"
      "s_mov_b64 exec, 1\n"
      "ds_add_u32 v253, v238 offset:96\n"
      "s_mov_b64 exec, -1\n"
      "SyG11_%=:\n"
      "F16_%=:\n"
      "ds_read_b32 v238, v253 offset:96\n"
      "s_waitcnt lgkmcnt(0)\n"
      "v_readfirstlane_b32 s51, v238\n"
      "s_cmp_eq_u32 s51, 4\n"
      "s_cbranch_scc1 F17_%=\n"
      "s_sleep 1\n"
      "s_sub_u32 s92, s92, 1\n"
      "s_cbranch_scc0 F16_%=\n"
      "F17_%=:\n"
      "s_setprio 1\n"
      "ds_read_b128 v[230:233], v249 offset:0\n"
      "ds_read_b128 v[226:229], v247 offset:0\n"
      "ds_read_b64_tr_b16 v[16:17], v252 offset:0\n"
      "ds_read_b64_tr_b16 v[18:19], v252 offset:2304\n"
      "ds_read_b128 v[222:225], v248 offset:0\n"
      "ds_read_b128 v[218:221], v246 offset:0\n"
      "ds_read_b64_tr_b16 v[8:9], v252 offset:9216\n"
      "ds_read_b64_tr_b16 v[10:11], v252 offset:11520\n"
      "ds_read_b128 v[206:209], v249 offset:64\n"
      "ds_read_b128 v[92:95], v247 offset:64\n"
      "ds_read_b64_tr_b16 v[4:5], v252 offset:18432\n"
      "ds_read_b64_tr_b16 v[6:7], v252 offset:20736\n"
      "ds_read_b128 v[84:87], v248 offset:64\n"
      "ds_read_b128 v[88:91], v246 offset:64\n"
      "s_waitcnt lgkmcnt(10)\n"
      "v_mfma_f32_32x32x16_bf16 v[52:67], v[16:19], v[230:233], 0\n"
      "v_mfma_f32_32x32x16_bf16 v[20:35], v[16:19], v[226:229], 0\n"
      "ds_read_b64_tr_b16 v[12:13], v252 offset:27648\n"
      "ds_read_b64_tr_b16 v[14:15], v252 offset:29952\n"
      "ds_read_b128 v[96:99], v249 offset:128\n"
      "ds_read_b128 v[202:205], v247 offset:128\n"
      "s_waitcnt lgkmcnt(10)\n"
      "v_mfma_f32_32x32x16_bf16 v[52:67], v[8:11], v[222:225], v[52:67]\n"
      "v_mfma_f32_32x32x16_bf16 v[20:35], v[8:11], v[218:221], v[20:35]\n"
      "ds_read_b64_tr_b16 v[16:17], v252 offset:36864\n"
      "ds_read_b64_tr_b16 v[18:19], v252 offset:39168\n"
      "ds_read_b128 v[210:213], v248 offset:128\n"
      "ds_read_b128 v[214:217], v246 offset:128\n"
      "s_waitcnt lgkmcnt(10)\n"
      "v_mfma_f32_32x32x16_bf16 v[52:67], v[4:7], v[206:209], v[52:67]\n"
      "v_mfma_f32_32x32x16_bf16 v[20:35], v[4:7], v[92:95], v[20:35]\n"
      "ds_read_b64_tr_b16 v[8:9], v252 offset:46080\n"
      "ds_read_b64_tr_b16 v[10:11], v252 offset:48384\n"
      "s_waitcnt lgkmcnt(8)\n"
      "v_mfma_f32_32x32x16_bf16 v[52:67], v[12:15], v[84:87], v[52:67]\n"
      "v_mfma_f32_32x32x16_bf16 v[20:35], v[12:15], v[88:91], v[20:35]\n"
      "ds_read_b64_tr_b16 v[72:73], v251 offset:0\n"
      "ds_read_b64_tr_b16 v[74:75], v251 offset:2304\n"
      "s_waitcnt lgkmcnt(6)\n"
      "v_mfma_f32_32x32x16_bf16 v[52:67], v[16:19], v[96:99], v[52:67]\n"
      "v_mfma_f32_32x32x16_bf16 v[20:35], v[16:19], v[202:205], v[20:35]\n"
      "ds_read_b64_tr_b16 v[76:77], v251 offset:9216\n"
      "ds_read_b64_tr_b16 v[78:79], v251 offset:11520\n"
      "s_cmp_eq_u32 s27, 0\n"
      "s_cbranch_scc1 Wo19_%=\n"
      "s_cmp_lg_u32 s25, 0\n"
      "s_cbranch_scc1 W20_%=a\n"
      "s_waitcnt vmcnt(3)\n"
      "s_branch W20_%=b\n"
      "W20_%=a:\n"
      "s_waitcnt vmcnt(2)\n"
      "W20_%=b:\n"
      "v_mov_b32 v243, 1\n"
      "s_mov_b64 exec, 1\n"
      "ds_add_u32 v253, v243 offset:100\n"
      "s_mov_b64 exec, -1\n"
      "Wo19_%=:\n"
      "s_waitcnt lgkmcnt(4)\n"
      "v_mfma_f32_32x32x16_bf16 v[52:67], v[8:11], v[210:213], v[52:67]\n"
      "v_mfma_f32_32x32x16_bf16 v[20:35], v[8:11], v[214:217], v[20:35]\n"
      "ds_read_b64_tr_b16 v[80:81], v251 offset:18432\n"
      "ds_read_b64_tr_b16 v[82:83], v251 offset:20736\n"
      "s_waitcnt lgkmcnt(4)\n"
      "v_mfma_f32_32x32x16_bf16 v[36:51], v[72:75], v[230:233], 0\n"
      "s_nop 6\n"
      "v_mul_f32 v52, 0x3e5105ec, v52\n"
      "v_mul_f32 v53, 0x3e5105ec, v53\n"
      "v_cvt_pk_bf16_f32 v100, v52, v53\n"
      "v_mul_f32 v54, 0x3e5105ec, v54\n"
      "v_mul_f32 v55, 0x3e5105ec, v55\n"
      "v_mfma_f32_32x32x16_bf16 v[4:19], v[72:75], v[226:229], 0\n"
      "ds_read_b64_tr_b16 v[68:69], v251 offset:27648\n"
      "ds_read_b64_tr_b16 v[70:71], v251 offset:29952\n"
      "v_cvt_pk_bf16_f32 v101, v54, v55\n"
      "v_mul_f32 v56, 0x3e5105ec, v56\n"
      "v_mul_f32 v57, 0x3e5105ec, v57\n"
      "s_waitcnt lgkmcnt(4)\n"
      "v_mfma_f32_32x32x16_bf16 v[36:51], v[76:79], v[222:225], v[36:51]\n"
      "v_cvt_pk_bf16_f32 v102, v56, v57\n"
      "v_mul_f32 v58, 0x3e5105ec, v58\n"
      "v_mul_f32 v59, 0x3e5105ec, v59\n"
      "v_cvt_pk_bf16_f32 v103, v58, v59\n"
      "v_mul_f32 v60, 0x3e5105ec, v60\n"
      "v_mfma_f32_32x32x16_bf16 v[4:19], v[76:79], v[218:221], v[4:19]\n"
      "ds_read_b64_tr_b16 v[72:73], v251 offset:36864\n"
      "ds_read_b64_tr_b16 v[74:75], v251 offset:39168\n"
      "v_mul_f32 v61, 0x3e5105ec, v61\n"
      "v_cvt_pk_bf16_f32 v104, v60, v61\n"
      "v_mul_f32 v62, 0x3e5105ec, v62\n"
      "s_waitcnt lgkmcnt(4)\n"
      "v_mfma_f32_32x32x16_bf16 v[36:51], v[80:83], v[206:209], v[36:51]\n"
      "v_mul_f32 v63, 0x3e5105ec, v63\n"
      "v_cvt_pk_bf16_f32 v105, v62, v63\n"
      "v_mul_f32 v64, 0x3e5105ec, v64\n"
      "v_mul_f32 v65, 0x3e5105ec, v65\n"
      "v_cvt_pk_bf16_f32 v106, v64, v65\n"
      "v_mfma_f32_32x32x16_bf16 v[4:19], v[80:83], v[92:95], v[4:19]\n"
      "ds_read_b64_tr_b16 v[76:77], v251 offset:46080\n"
      "ds_read_b64_tr_b16 v[78:79], v251 offset:48384\n"
      "v_mul_f32 v66, 0x3e5105ec, v66\n"
      "v_mul_f32 v67, 0x3e5105ec, v67\n"
      "v_cvt_pk_bf16_f32 v107, v66, v67\n"
      "s_waitcnt lgkmcnt(4)\n"
      "v_mfma_f32_32x32x16_bf16 v[36:51], v[68:71], v[84:87], v[36:51]\n"
      "v_mul_f32 v20, 0x3e5105ec, v20\n"
      "v_mul_f32 v21, 0x3e5105ec, v21\n"
      "v_cvt_pk_bf16_f32 v116, v20, v21\n"
      "v_mul_f32 v22, 0x3e5105ec, v22\n"
      "v_mul_f32 v23, 0x3e5105ec, v23\n"
      "v_mfma_f32_32x32x16_bf16 v[4:19], v[68:71], v[88:91], v[4:19]\n"
      "ds_read_b64_tr_b16 v[80:81], v252 offset:192\n"
      "ds_read_b64_tr_b16 v[82:83], v252 offset:2496\n"
      "v_cvt_pk_bf16_f32 v117, v22, v23\n"
      "v_mul_f32 v24, 0x3e5105ec, v24\n"
      "v_mul_f32 v25, 0x3e5105ec, v25\n"
      "s_waitcnt lgkmcnt(4)\n"
      "v_mfma_f32_32x32x16_bf16 v[36:51], v[72:75], v[96:99], v[36:51]\n"
      "v_cvt_pk_bf16_f32 v118, v24, v25\n"
      "v_mul_f32 v26, 0x3e5105ec, v26\n"
      "v_mul_f32 v27, 0x3e5105ec, v27\n"
      "v_cvt_pk_bf16_f32 v119, v26, v27\n"
      "v_mul_f32 v28, 0x3e5105ec, v28\n"
      "v_mfma_f32_32x32x16_bf16 v[4:19], v[72:75], v[202:205], v[4:19]\n"
      "ds_read_b64_tr_b16 v[68:69], v252 offset:9408\n"
      "ds_read_b64_tr_b16 v[70:71], v252 offset:11712\n"
      "v_mul_f32 v29, 0x3e5105ec, v29\n"
      "v_cvt_pk_bf16_f32 v120, v28, v29\n"
      "v_mul_f32 v30, 0x3e5105ec, v30\n"
      "s_waitcnt lgkmcnt(4)\n"
      "v_mfma_f32_32x32x16_bf16 v[36:51], v[76:79], v[210:213], v[36:51]\n"
      "v_mul_f32 v31, 0x3e5105ec, v31\n"
      "v_cvt_pk_bf16_f32 v121, v30, v31\n"
      "v_mul_f32 v32, 0x3e5105ec, v32\n"
      "v_mul_f32 v33, 0x3e5105ec, v33\n"
      "v_cvt_pk_bf16_f32 v122, v32, v33\n"
      "v_mfma_f32_32x32x16_bf16 v[4:19], v[76:79], v[214:217], v[4:19]\n"
      "ds_read_b64_tr_b16 v[72:73], v252 offset:18624\n"
      "ds_read_b64_tr_b16 v[74:75], v252 offset:20928\n"
      "v_mul_f32 v34, 0x3e5105ec, v34\n"
      "v_mul_f32 v35, 0x3e5105ec, v35\n"
      "v_cvt_pk_bf16_f32 v123, v34, v35\n"
      "s_waitcnt lgkmcnt(4)\n"
      "v_mfma_f32_32x32x16_bf16 v[52:67], v[80:83], v[230:233], 0\n"
      "v_mul_f32 v36, 0x3e5105ec, v36\n"
      "v_mul_f32 v37, 0x3e5105ec, v37\n"
      "v_cvt_pk_bf16_f32 v108, v36, v37\n"
      "v_mul_f32 v38, 0x3e5105ec, v38\n"
      "v_mul_f32 v39, 0x3e5105ec, v39\n"
      "v_mfma_f32_32x32x16_bf16 v[20:35], v[80:83], v[226:229], 0\n"
      "ds_read_b64_tr_b16 v[76:77], v252 offset:27840\n"
      "ds_read_b64_tr_b16 v[78:79], v252 offset:30144\n"
      "v_cvt_pk_bf16_f32 v109, v38, v39\n"
      "v_mul_f32 v40, 0x3e5105ec, v40\n"
      "v_mul_f32 v41, 0x3e5105ec, v41\n"
      "s_waitcnt lgkmcnt(4)\n"
      "v_mfma_f32_32x32x16_bf16 v[52:67], v[68:71], v[222:225], v[52:67]\n"
      "v_cvt_pk_bf16_f32 v112, v40, v41\n"
      "v_mul_f32 v42, 0x3e5105ec, v42\n"
      "v_mul_f32 v43, 0x3e5105ec, v43\n"
      "v_cvt_pk_bf16_f32 v113, v42, v43\n"
      "v_cvt_pk_bf16_f32 v194, v44, v45\n"
      "v_mfma_f32_32x32x16_bf16 v[20:35], v[68:71], v[218:221], v[20:35]\n"
      "ds_read_b64_tr_b16 v[80:81], v252 offset:37056\n"
      "ds_read_b64_tr_b16 v[82:83], v252 offset:39360\n"
      "v_cvt_pk_bf16_f32 v195, v46, v47\n"
      "v_cvt_pk_bf16_f32 v196, v48, v49\n"
      "v_cvt_pk_bf16_f32 v197, v50, v51\n"
      "s_waitcnt lgkmcnt(4)\n"
      "v_mfma_f32_32x32x16_bf16 v[52:67], v[72:75], v[206:209], v[52:67]\n"
      "v_mul_f32 v4, 0x3e5105ec, v4\n"
      "v_mul_f32 v5, 0x3e5105ec, v5\n"
      "v_cvt_pk_bf16_f32 v140, v4, v5\n"
      "v_mul_f32 v6, 0x3e5105ec, v6\n"
      "v_mul_f32 v7, 0x3e5105ec, v7\n"
      "v_mfma_f32_32x32x16_bf16 v[20:35], v[72:75], v[92:95], v[20:35]\n"
      "ds_read_b64_tr_b16 v[68:69], v252 offset:46272\n"
      "ds_read_b64_tr_b16 v[70:71], v252 offset:48576\n"
      "v_cvt_pk_bf16_f32 v141, v6, v7\n"
      "v_mul_f32 v8, 0x3e5105ec, v8\n"
      "v_mul_f32 v9, 0x3e5105ec, v9\n"
      "s_waitcnt lgkmcnt(4)\n"
      "v_mfma_f32_32x32x16_bf16 v[52:67], v[76:79], v[84:87], v[52:67]\n"
      "v_cvt_pk_bf16_f32 v144, v8, v9\n"
      "v_mul_f32 v10, 0x3e5105ec, v10\n"
      "v_mul_f32 v11, 0x3e5105ec, v11\n"
      "v_cvt_pk_bf16_f32 v145, v10, v11\n"
      "v_cvt_pk_bf16_f32 v198, v12, v13\n"
      "v_mfma_f32_32x32x16_bf16 v[20:35], v[76:79], v[88:91], v[20:35]\n"
      "ds_read_b64_tr_b16 v[132:133], v250 offset:0\n"
      "ds_read_b64_tr_b16 v[134:135], v250 offset:2304\n"
      "v_cvt_pk_bf16_f32 v199, v14, v15\n"
      "v_cvt_pk_bf16_f32 v200, v16, v17\n"
      "v_cvt_pk_bf16_f32 v201, v18, v19\n"
      "s_waitcnt lgkmcnt(4)\n"
      "v_mfma_f32_32x32x16_bf16 v[52:67], v[80:83], v[96:99], v[52:67]\n"
      "s_cmp_lg_u32 s25, 0\n"
      "s_cbranch_scc1 Sw18_%=\n"
      "v_swap_b32 v100, v116\n"
      "v_swap_b32 v101, v117\n"
      "v_swap_b32 v102, v118\n"
      "v_swap_b32 v103, v119\n"
      "v_swap_b32 v104, v120\n"
      "v_swap_b32 v105, v121\n"
      "v_swap_b32 v106, v122\n"
      "v_swap_b32 v107, v123\n"
      "v_swap_b32 v108, v140\n"
      "v_swap_b32 v109, v141\n"
      "v_swap_b32 v112, v144\n"
      "v_swap_b32 v113, v145\n"
      "Sw18_%=:\n"
      "v_mfma_f32_32x32x16_bf16 v[20:35], v[80:83], v[202:205], v[20:35]\n"
      "ds_read_b64_tr_b16 v[234:235], v250 offset:9216\n"
      "ds_read_b64_tr_b16 v[236:237], v250 offset:11520\n"
      "s_waitcnt lgkmcnt(4)\n"
      "v_mfma_f32_32x32x16_bf16 v[52:67], v[68:71], v[210:213], v[52:67]\n"
      "v_mfma_f32_32x32x16_bf16 v[20:35], v[68:71], v[214:217], v[20:35]\n"
      "ds_read_b64_tr_b16 v[4:5], v250 offset:18432\n"
      "ds_read_b64_tr_b16 v[6:7], v250 offset:20736\n"
      "s_waitcnt lgkmcnt(4)\n"
      "v_mfma_f32_32x32x16_bf16 v[36:51], v[230:233], v[132:135], 0\n"
      "s_nop 6\n"
      "v_cvt_pk_bf16_f32 v124, v52, v53\n"
      "v_cvt_pk_bf16_f32 v125, v54, v55\n"
      "v_cvt_pk_bf16_f32 v126, v56, v57\n"
      "v_cvt_pk_bf16_f32 v127, v58, v59\n"
      "v_cvt_pk_bf16_f32 v128, v60, v61\n"
      "v_mfma_f32_32x32x16_bf16 v[68:83], v[226:229], v[132:135], 0\n"
      "ds_read_b64_tr_b16 v[8:9], v250 offset:27648\n"
      "ds_read_b64_tr_b16 v[10:11], v250 offset:29952\n"
      "v_cvt_pk_bf16_f32 v129, v62, v63\n"
      "v_cvt_pk_bf16_f32 v130, v64, v65\n"
      "v_cvt_pk_bf16_f32 v131, v66, v67\n"
      "s_waitcnt lgkmcnt(4)\n"
      "v_mfma_f32_32x32x16_bf16 v[36:51], v[222:225], v[234:237], v[36:51]\n"
      "v_cvt_pk_bf16_f32 v132, v20, v21\n"
      "v_cvt_pk_bf16_f32 v133, v22, v23\n"
      "v_cvt_pk_bf16_f32 v134, v24, v25\n"
      "v_cvt_pk_bf16_f32 v135, v26, v27\n"
      "v_cvt_pk_bf16_f32 v136, v28, v29\n"
      "v_mfma_f32_32x32x16_bf16 v[68:83], v[218:221], v[234:237], v[68:83]\n"
      "ds_read_b64_tr_b16 v[12:13], v250 offset:36864\n"
      "ds_read_b64_tr_b16 v[14:15], v250 offset:39168\n"
      "v_cvt_pk_bf16_f32 v137, v30, v31\n"
      "v_cvt_pk_bf16_f32 v138, v32, v33\n"
      "v_cvt_pk_bf16_f32 v139, v34, v35\n"
      "s_waitcnt lgkmcnt(4)\n"
      "v_mfma_f32_32x32x16_bf16 v[36:51], v[206:209], v[4:7], v[36:51]\n"
      "v_mfma_f32_32x32x16_bf16 v[68:83], v[92:95], v[4:7], v[68:83]\n"
      "ds_read_b64_tr_b16 v[234:235], v250 offset:46080\n"
      "ds_read_b64_tr_b16 v[236:237], v250 offset:48384\n"
      "s_waitcnt lgkmcnt(4)\n"
      "v_mfma_f32_32x32x16_bf16 v[36:51], v[84:87], v[8:11], v[36:51]\n"
      "v_mfma_f32_32x32x16_bf16 v[68:83], v[88:91], v[8:11], v[68:83]\n"
      "ds_read_b64_tr_b16 v[60:61], v250 offset:48\n"
      "ds_read_b64_tr_b16 v[62:63], v250 offset:2352\n"
      "s_waitcnt lgkmcnt(4)\n"
      "v_mfma_f32_32x32x16_bf16 v[36:51], v[96:99], v[12:15], v[36:51]\n"
      "v_mfma_f32_32x32x16_bf16 v[68:83], v[202:205], v[12:15], v[68:83]\n"
      "ds_read_b64_tr_b16 v[64:65], v250 offset:9264\n"
      "ds_read_b64_tr_b16 v[66:67], v250 offset:11568\n"
      "s_waitcnt lgkmcnt(4)\n"
      "v_mfma_f32_32x32x16_bf16 v[36:51], v[210:213], v[234:237], v[36:51]\n"
      "v_mfma_f32_32x32x16_bf16 v[68:83], v[214:217], v[234:237], v[68:83]\n"
      "ds_read_b64_tr_b16 v[56:57], v250 offset:18480\n"
      "ds_read_b64_tr_b16 v[58:59], v250 offset:20784\n"
      "s_waitcnt lgkmcnt(4)\n"
      "v_mfma_f32_32x32x16_bf16 v[4:19], v[230:233], v[60:63], 0\n"
      "s_nop 6\n"
      "s_mov_b64 exec, s[94:95]\n"
      "v_cvt_pk_bf16_f32 v182, v36, v37\n"
      "v_cvt_pk_bf16_f32 v183, v38, v39\n"
      "v_cvt_pk_bf16_f32 v184, v40, v41\n"
      "v_cvt_pk_bf16_f32 v185, v42, v43\n"
      "s_mov_b64 exec, -1\n"
      "v_mfma_f32_32x32x16_bf16 v[20:35], v[226:229], v[60:63], 0\n"
      "ds_read_b64_tr_b16 v[52:53], v250 offset:27696\n"
      "ds_read_b64_tr_b16 v[54:55], v250 offset:30000\n"
      "s_mov_b64 exec, s[94:95]\n"
      "v_cvt_pk_bf16_f32 v174, v44, v45\n"
      "v_cvt_pk_bf16_f32 v175, v46, v47\n"
      "v_cvt_pk_bf16_f32 v176, v48, v49\n"
      "v_cvt_pk_bf16_f32 v177, v50, v51\n"
      "s_mov_b64 exec, -1\n"
      "s_waitcnt lgkmcnt(4)\n"
      "v_mfma_f32_32x32x16_bf16 v[4:19], v[222:225], v[64:67], v[4:19]\n"
      "s_mov_b64 exec, s[94:95]\n"
      "v_cvt_pk_bf16_f32 v170, v68, v69\n"
      "v_cvt_pk_bf16_f32 v171, v70, v71\n"
      "v_cvt_pk_bf16_f32 v172, v72, v73\n"
      "v_cvt_pk_bf16_f32 v173, v74, v75\n"
      "v_cvt_pk_bf16_f32 v166, v76, v77\n"
      "s_mov_b64 exec, -1\n"
      "v_mfma_f32_32x32x16_bf16 v[20:35], v[218:221], v[64:67], v[20:35]\n"
      "ds_read_b64_tr_b16 v[60:61], v250 offset:36912\n"
      "ds_read_b64_tr_b16 v[62:63], v250 offset:39216\n"
      "s_lshl_b32 s40, s25, 5\n"
      "v_add_u32 v243, s40, v2\n"
      "v_min_u32 v243, 48, v243\n"
      "s_waitcnt lgkmcnt(4)\n"
      "v_mfma_f32_32x32x16_bf16 v[4:19], v[206:209], v[56:59], v[4:19]\n"
      "s_mul_i32 s40, s26, 4802\n"
      "s_add_u32 s40, s40, s33\n"
      "s_add_u32 s40, s40, s7\n"
      "s_add_u32 s40, s40, 111360\n"
      "s_mul_i32 s41, s25, 10976\n"
      "s_add_u32 s41, s41, s7\n"
      "s_add_u32 s41, s41, 130944\n"
      "v_mul_u32_u24 v236, 98, v243\n"
      "v_add_u32 v236, s40, v236\n"
      "v_lshl_add_u32 v235, v3, 3, v236\n"
      "v_mul_u32_u24 v237, 112, v243\n"
      "v_mfma_f32_32x32x16_bf16 v[20:35], v[92:95], v[56:59], v[20:35]\n"
      "ds_read_b64_tr_b16 v[36:37], v250 offset:46128\n"
      "ds_read_b64_tr_b16 v[38:39], v250 offset:48432\n"
      "v_add_u32 v237, s41, v237\n"
      "v_lshl_add_u32 v234, v3, 4, v237\n"
      "v_lshl_add_u32 v237, v3, 2, v237\n"
      "s_waitcnt lgkmcnt(4)\n"
      "v_mfma_f32_32x32x16_bf16 v[4:19], v[84:87], v[52:55], v[4:19]\n"
      "v_mfma_f32_32x32x16_bf16 v[20:35], v[88:91], v[52:55], v[20:35]\n"
      "s_waitcnt lgkmcnt(2)\n"
      "v_mfma_f32_32x32x16_bf16 v[4:19], v[96:99], v[60:63], v[4:19]\n"
      "v_mfma_f32_32x32x16_bf16 v[20:35], v[202:205], v[60:63], v[20:35]\n"
      "s_waitcnt lgkmcnt(0)\n"
      "v_mfma_f32_32x32x16_bf16 v[4:19], v[210:213], v[36:39], v[4:19]\n"
      "v_mfma_f32_32x32x16_bf16 v[20:35], v[214:217], v[36:39], v[20:35]\n"
      "s_waitcnt vmcnt(0)\n"
      "v_mov_b32 v238, 1\n"
      "ds_write_b32 v255, v238 offset:32\n"
      "F21_%=:\n"
      "ds_read_b32 v238, v254 offset:32\n"
      "s_waitcnt lgkmcnt(0)\n"
      "v_readfirstlane_b32 s51, v238\n"
      "s_cmp_lg_u32 s51, 0\n"
      "s_cbranch_scc1 F22_%=\n"
      "s_sleep 1\n"
      "s_sub_u32 s92, s92, 1\n"
      "s_cbranch_scc0 F21_%=\n"
      "F22_%=:\n"
      "s_setprio 0\n"
      "ds_read_b128 v[218:221], v240 offset:0\n"
      "ds_read_b128 v[206:209], v240 offset:32\n"
      "ds_read_b128 v[222:225], v240 offset:64\n"
      "ds_read_b32 v233, v242 offset:96\n"
      "ds_read_u16_d16_hi v68, v239 offset:0\n"
      "ds_read_u16_d16_hi v69, v239 offset:2\n"
      "ds_read_u16_d16_hi v70, v239 offset:4\n"
      "ds_read_u16_d16_hi v71, v239 offset:6\n"
      "ds_read_u16_d16_hi v72, v239 offset:16\n"
      "ds_read_u16_d16_hi v73, v239 offset:18\n"
      "ds_read_u16_d16_hi v74, v239 offset:20\n"
      "ds_read_u16_d16_hi v75, v239 offset:22\n"
      "ds_read_u16_d16_hi v76, v239 offset:32\n"
      "ds_read_u16_d16_hi v77, v239 offset:34\n"
      "ds_read_u16_d16_hi v78, v239 offset:36\n"
      "ds_read_u16_d16_hi v79, v239 offset:38\n"
      "ds_read_u16_d16_hi v80, v239 offset:48\n"
      "ds_read_u16_d16_hi v81, v239 offset:50\n"
      "ds_read_u16_d16_hi v82, v239 offset:52\n"
      "ds_read_u16_d16_hi v83, v239 offset:54\n"
      "ds_read_u16_d16_hi v84, v239 offset:64\n"
      "ds_read_u16_d16_hi v85, v239 offset:66\n"
      "ds_read_u16_d16_hi v86, v239 offset:68\n"
      "ds_read_u16_d16_hi v87, v239 offset:70\n"
      "ds_read_u16_d16_hi v88, v239 offset:80\n"
      "ds_read_u16_d16_hi v89, v239 offset:82\n"
      "ds_read_u16_d16_hi v90, v239 offset:84\n"
      "ds_read_u16_d16_hi v91, v239 offset:86\n"
      "ds_read_u16_d16_hi v92, v241 offset:96\n"
      "ds_read_b128 v[210:213], v240 offset:5488\n"
      "ds_read_b128 v[202:205], v240 offset:5520\n"
      "ds_read_b128 v[214:217], v240 offset:5552\n"
      "ds_read_b32 v238, v242 offset:5584\n"
      "s_mov_b64 exec, s[94:95]\n"
      "v_cvt_pk_bf16_f32 v162, v4, v5\n"
      "v_cvt_pk_bf16_f32 v163, v6, v7\n"
      "v_cvt_pk_bf16_f32 v164, v8, v9\n"
      "v_cvt_pk_bf16_f32 v165, v10, v11\n"
      "s_mov_b64 exec, -1\n"
      "s_mov_b64 exec, s[94:95]\n"
      "v_cvt_pk_bf16_f32 v158, v12, v13\n"
      "v_cvt_pk_bf16_f32 v159, v14, v15\n"
      "v_cvt_pk_bf16_f32 v160, v16, v17\n"
      "v_cvt_pk_bf16_f32 v161, v18, v19\n"
      "s_mov_b64 exec, -1\n"
      "s_mov_b64 exec, s[94:95]\n"
      "v_cvt_pk_bf16_f32 v150, v20, v21\n"
      "v_cvt_pk_bf16_f32 v151, v22, v23\n"
      "v_cvt_pk_bf16_f32 v152, v24, v25\n"
      "v_cvt_pk_bf16_f32 v153, v26, v27\n"
      "v_cvt_pk_bf16_f32 v146, v28, v29\n"
      "s_mov_b64 exec, -1\n"
      "s_setprio 2\n"
      "s_waitcnt lgkmcnt(13)\n"
      "v_mfma_f32_32x32x16_bf16 v[36:51], v[190:193], v[218:221], v[68:83]\n"
      "s_waitcnt lgkmcnt(4)\n"
      "v_mfma_f32_32x32x16_bf16 v[52:67], v[190:193], v[222:225], v[84:99]\n"
      "v_mfma_f32_32x32x16_bf16 v[36:51], v[186:189], v[206:209], v[36:51]\n"
      "v_mfma_f32_32x32x16_bf16 v[52:67], v[132:135], v[100:103], v[52:67]\n"
      "v_mfma_f32_32x32x16_bf16 v[36:51], v[124:127], v[100:103], v[36:51]\n"
      "v_mfma_f32_32x32x16_bf16 v[52:67], v[198:201], v[108:111], v[52:67]\n"
      "v_mfma_f32_32x32x16_bf16 v[36:51], v[194:197], v[108:111], v[36:51]\n"
      "s_waitcnt lgkmcnt(3)\n"
      "v_mfma_f32_32x32x16_bf16 v[68:83], v[190:193], v[210:213], v[68:83]\n"
      "s_nop 8\n"
      "v_add_f32 v60, v60, v233\n"
      "s_waitcnt lgkmcnt(1)\n"
      "v_mfma_f32_32x32x16_bf16 v[84:99], v[190:193], v[214:217], v[84:99]\n"
      "v_max3_f32 v242, v36, v37, v38\n"
      "v_max3_f32 v241, v39, v40, v41\n"
      "v_max3_f32 v240, v42, v43, v44\n"
      "v_max3_f32 v242, v242, v45, v46\n"
      "v_max3_f32 v241, v241, v47, v48\n"
      "v_mfma_f32_32x32x16_bf16 v[68:83], v[186:189], v[202:205], v[68:83]\n"
      "v_max3_f32 v240, v240, v49, v50\n"
      "v_max3_f32 v242, v242, v51, v52\n"
      "v_max3_f32 v241, v241, v53, v54\n"
      "v_max3_f32 v240, v240, v55, v56\n"
      "v_max3_f32 v242, v242, v57, v58\n"
      "v_mfma_f32_32x32x16_bf16 v[84:99], v[136:139], v[104:107], v[84:99]\n"
      "v_max3_f32 v241, v241, v59, v60\n"
      "v_max3_f32 v242, v242, v241, v240\n"
      "v_mov_b32 v241, v242\n"
      "s_nop 1\n"
      "v_permlane32_swap_b32 v242, v241\n"
      "v_mfma_f32_32x32x16_bf16 v[68:83], v[128:131], v[104:107], v[68:83]\n"
      "v_max_f32 v242, v242, v241\n"
      "v_mul_f32 v242, 0xbfb8aa3b, v242\n"
      "v_fma_f32 v36, v36, s48, v242\n"
      "v_fma_f32 v37, v37, s48, v242\n"
      "v_fma_f32 v38, v38, s48, v242\n"
      "v_mfma_f32_32x32x16_bf16 v[84:99], v[198:201], v[110:113], v[84:99]\n"
      "v_exp_f32 v36, v36\n"
      "v_fma_f32 v39, v39, s48, v242\n"
      "v_exp_f32 v37, v37\n"
      "v_mfma_f32_32x32x16_bf16 v[68:83], v[194:197], v[110:113], v[68:83]\n"
      "s_setprio 0\n"
      "ds_read_b128 v[222:225], v234 offset:0\n"
      "ds_read_b128 v[206:209], v234 offset:32\n"
      "ds_read_b128 v[218:221], v234 offset:64\n"
      "ds_read_b32 v239, v237 offset:96\n"
      "ds_read_u16_d16_hi v4, v235 offset:0\n"
      "ds_read_u16_d16_hi v5, v235 offset:2\n"
      "ds_read_u16_d16_hi v6, v235 offset:4\n"
      "ds_read_u16_d16_hi v7, v235 offset:6\n"
      "ds_read_u16_d16_hi v8, v235 offset:16\n"
      "ds_read_u16_d16_hi v9, v235 offset:18\n"
      "ds_read_u16_d16_hi v10, v235 offset:20\n"
      "ds_read_u16_d16_hi v11, v235 offset:22\n"
      "ds_read_u16_d16_hi v12, v235 offset:32\n"
      "ds_read_u16_d16_hi v13, v235 offset:34\n"
      "ds_read_u16_d16_hi v14, v235 offset:36\n"
      "ds_read_u16_d16_hi v15, v235 offset:38\n"
      "ds_read_u16_d16_hi v16, v235 offset:48\n"
      "ds_read_u16_d16_hi v17, v235 offset:50\n"
      "ds_read_u16_d16_hi v18, v235 offset:52\n"
      "ds_read_u16_d16_hi v19, v235 offset:54\n"
      "ds_read_u16_d16_hi v20, v235 offset:64\n"
      "ds_read_u16_d16_hi v21, v235 offset:66\n"
      "ds_read_u16_d16_hi v22, v235 offset:68\n"
      "ds_read_u16_d16_hi v23, v235 offset:70\n"
      "ds_read_u16_d16_hi v24, v235 offset:80\n"
      "ds_read_u16_d16_hi v25, v235 offset:82\n"
      "ds_read_u16_d16_hi v26, v235 offset:84\n"
      "ds_read_u16_d16_hi v27, v235 offset:86\n"
      "ds_read_u16_d16_hi v28, v236 offset:96\n"
      "ds_read_b128 v[214:217], v234 offset:5488\n"
      "ds_read_b128 v[202:205], v234 offset:5520\n"
      "ds_read_b128 v[210:213], v234 offset:5552\n"
      "ds_read_b32 v241, v237 offset:5584\n"
      "v_fma_f32 v40, v40, s48, v242\n"
      "v_exp_f32 v38, v38\n"
      "v_cvt_pk_bf16_f32 v36, v36, v37\n"
      "v_fma_f32 v41, v41, s48, v242\n"
      "v_exp_f32 v39, v39\n"
      "v_fma_f32 v42, v42, s48, v242\n"
      "v_exp_f32 v40, v40\n"
      "v_cvt_pk_bf16_f32 v37, v38, v39\n"
      "v_fma_f32 v43, v43, s48, v242\n"
      "v_exp_f32 v41, v41\n"
      "v_fma_f32 v44, v44, s48, v242\n"
      "v_exp_f32 v42, v42\n"
      "v_cvt_pk_bf16_f32 v38, v40, v41\n"
      "v_fma_f32 v45, v45, s48, v242\n"
      "v_exp_f32 v43, v43\n"
      "v_fma_f32 v46, v46, s48, v242\n"
      "v_exp_f32 v44, v44\n"
      "v_cvt_pk_bf16_f32 v39, v42, v43\n"
      "v_fma_f32 v47, v47, s48, v242\n"
      "s_setprio 2\n"
      "v_mfma_f32_32x32x16_bf16 v[100:115], v[182:185], v[36:39], 0\n"
      "s_setprio 0\n"
      "v_exp_f32 v45, v45\n"
      "v_fma_f32 v48, v48, s48, v242\n"
      "v_exp_f32 v46, v46\n"
      "v_cvt_pk_bf16_f32 v40, v44, v45\n"
      "v_fma_f32 v49, v49, s48, v242\n"
      "v_exp_f32 v47, v47\n"
      "v_fma_f32 v50, v50, s48, v242\n"
      "v_exp_f32 v48, v48\n"
      "v_cvt_pk_bf16_f32 v41, v46, v47\n"
      "v_fma_f32 v51, v51, s48, v242\n"
      "v_exp_f32 v49, v49\n"
      "v_fma_f32 v52, v52, s48, v242\n"
      "v_exp_f32 v50, v50\n"
      "v_cvt_pk_bf16_f32 v42, v48, v49\n"
      "v_fma_f32 v53, v53, s48, v242\n"
      "v_exp_f32 v51, v51\n"
      "v_fma_f32 v54, v54, s48, v242\n"
      "v_exp_f32 v52, v52\n"
      "v_cvt_pk_bf16_f32 v43, v50, v51\n"
      "v_fma_f32 v55, v55, s48, v242\n"
      "s_setprio 2\n"
      "v_mfma_f32_32x32x16_bf16 v[100:115], v[174:177], v[40:43], v[100:115]\n"
      "s_setprio 0\n"
      "v_exp_f32 v53, v53\n"
      "v_fma_f32 v56, v56, s48, v242\n"
      "v_exp_f32 v54, v54\n"
      "v_cvt_pk_bf16_f32 v44, v52, v53\n"
      "v_fma_f32 v57, v57, s48, v242\n"
      "v_exp_f32 v55, v55\n"
      "v_fma_f32 v58, v58, s48, v242\n"
      "v_exp_f32 v56, v56\n"
      "v_cvt_pk_bf16_f32 v45, v54, v55\n"
      "v_fma_f32 v59, v59, s48, v242\n"
      "v_exp_f32 v57, v57\n"
      "v_fma_f32 v60, v60, s48, v242\n"
      "v_exp_f32 v58, v58\n"
      "v_cvt_pk_bf16_f32 v46, v56, v57\n"
      "v_exp_f32 v59, v59\n"
      "v_exp_f32 v60, v60\n"
      "v_cvt_pk_bf16_f32 v47, v58, v59\n"
      "v_cvt_pk_bf16_f32 v178, v60, 0\n"
      "s_setprio 2\n"
      "v_mfma_f32_32x32x16_bf16 v[100:115], v[170:173], v[44:47], v[100:115]\n"
      "s_mul_i32 s42, s25, 9216\n"
      "s_mul_i32 s43, s35, 9216\n"
      "v_add_u32 v242, s42, v244\n"
      "v_add_u32 v240, s43, v244\n"
      "s_waitcnt lgkmcnt(15)\n"
      "v_add_f32 v92, v92, v238\n"
      "v_max3_f32 v237, v68, v69, v70\n"
      "v_mfma_f32_32x32x16_bf16 v[100:115], v[166:169], v[178:181], v[100:115]\n"
      "v_max3_f32 v236, v71, v72, v73\n"
      "v_max3_f32 v235, v74, v75, v76\n"
      "v_max3_f32 v237, v237, v77, v78\n"
      "v_max3_f32 v236, v236, v79, v80\n"
      "v_max3_f32 v235, v235, v81, v82\n"
      "s_waitcnt lgkmcnt(13)\n"
      "v_mfma_f32_32x32x16_bf16 v[36:51], v[190:193], v[222:225], v[4:19]\n"
      "v_max3_f32 v237, v237, v83, v84\n"
      "v_max3_f32 v236, v236, v85, v86\n"
      "v_max3_f32 v235, v235, v87, v88\n"
      "v_max3_f32 v237, v237, v89, v90\n"
      "v_max3_f32 v236, v236, v91, v92\n"
      "s_waitcnt lgkmcnt(4)\n"
      "v_mfma_f32_32x32x16_bf16 v[52:67], v[190:193], v[218:221], v[20:35]\n"
      "v_max3_f32 v237, v237, v236, v235\n"
      "v_mov_b32 v236, v237\n"
      "s_nop 1\n"
      "v_permlane32_swap_b32 v237, v236\n"
      "v_max_f32 v237, v237, v236\n"
      "v_mfma_f32_32x32x16_bf16 v[36:51], v[186:189], v[206:209], v[36:51]\n"
      "v_mul_f32 v237, 0xbfb8aa3b, v237\n"
      "v_fma_f32 v68, v68, s48, v237\n"
      "v_fma_f32 v69, v69, s48, v237\n"
      "v_fma_f32 v70, v70, s48, v237\n"
      "v_exp_f32 v68, v68\n"
      "v_mfma_f32_32x32x16_bf16 v[52:67], v[132:135], v[116:119], v[52:67]\n"
      "v_fma_f32 v71, v71, s48, v237\n"
      "v_exp_f32 v69, v69\n"
      "v_fma_f32 v72, v72, s48, v237\n"
      "v_exp_f32 v70, v70\n"
      "v_mfma_f32_32x32x16_bf16 v[36:51], v[124:127], v[116:119], v[36:51]\n"
      "v_cvt_pk_bf16_f32 v68, v68, v69\n"
      "v_fma_f32 v73, v73, s48, v237\n"
      "v_exp_f32 v71, v71\n"
      "v_fma_f32 v74, v74, s48, v237\n"
      "v_mfma_f32_32x32x16_bf16 v[52:67], v[198:201], v[140:143], v[52:67]\n"
      "v_exp_f32 v72, v72\n"
      "v_cvt_pk_bf16_f32 v69, v70, v71\n"
      "v_fma_f32 v75, v75, s48, v237\n"
      "v_exp_f32 v73, v73\n"
      "v_mfma_f32_32x32x16_bf16 v[36:51], v[194:197], v[140:143], v[36:51]\n"
      "v_fma_f32 v76, v76, s48, v237\n"
      "v_exp_f32 v74, v74\n"
      "v_cvt_pk_bf16_f32 v70, v72, v73\n"
      "v_fma_f32 v77, v77, s48, v237\n"
      "s_waitcnt lgkmcnt(3)\n"
      "v_mfma_f32_32x32x16_bf16 v[4:19], v[190:193], v[214:217], v[4:19]\n"
      "v_exp_f32 v75, v75\n"
      "v_fma_f32 v78, v78, s48, v237\n"
      "v_exp_f32 v76, v76\n"
      "s_waitcnt lgkmcnt(1)\n"
      "v_mfma_f32_32x32x16_bf16 v[20:35], v[190:193], v[210:213], v[20:35]\n"
      "v_cvt_pk_bf16_f32 v71, v74, v75\n"
      "v_fma_f32 v79, v79, s48, v237\n"
      "v_exp_f32 v77, v77\n"
      "v_fma_f32 v80, v80, s48, v237\n"
      "v_mfma_f32_32x32x16_bf16 v[4:19], v[186:189], v[202:205], v[4:19]\n"
      "v_exp_f32 v78, v78\n"
      "v_cvt_pk_bf16_f32 v72, v76, v77\n"
      "v_fma_f32 v81, v81, s48, v237\n"
      "v_exp_f32 v79, v79\n"
      "v_mfma_f32_32x32x16_bf16 v[20:35], v[136:139], v[120:123], v[20:35]\n"
      "v_fma_f32 v82, v82, s48, v237\n"
      "v_exp_f32 v80, v80\n"
      "v_cvt_pk_bf16_f32 v73, v78, v79\n"
      "v_fma_f32 v83, v83, s48, v237\n"
      "v_mfma_f32_32x32x16_bf16 v[4:19], v[128:131], v[120:123], v[4:19]\n"
      "v_exp_f32 v81, v81\n"
      "v_fma_f32 v84, v84, s48, v237\n"
      "v_exp_f32 v82, v82\n"
      "v_mfma_f32_32x32x16_bf16 v[20:35], v[198:201], v[142:145], v[20:35]\n"
      "v_cvt_pk_bf16_f32 v74, v80, v81\n"
      "v_fma_f32 v85, v85, s48, v237\n"
      "v_exp_f32 v83, v83\n"
      "v_fma_f32 v86, v86, s48, v237\n"
      "v_mfma_f32_32x32x16_bf16 v[4:19], v[194:197], v[142:145], v[4:19]\n"
      "v_exp_f32 v84, v84\n"
      "v_cvt_pk_bf16_f32 v75, v82, v83\n"
      "v_fma_f32 v87, v87, s48, v237\n"
      "v_exp_f32 v85, v85\n"
      "v_mfma_f32_32x32x16_bf16 v[116:131], v[162:165], v[68:71], 0\n"
      "v_fma_f32 v88, v88, s48, v237\n"
      "v_exp_f32 v86, v86\n"
      "v_cvt_pk_bf16_f32 v76, v84, v85\n"
      "v_fma_f32 v89, v89, s48, v237\n"
      "v_mfma_f32_32x32x16_bf16 v[116:131], v[158:161], v[72:75], v[116:131]\n"
      "v_exp_f32 v87, v87\n"
      "v_fma_f32 v90, v90, s48, v237\n"
      "v_exp_f32 v88, v88\n"
      "v_cvt_pk_bf16_f32 v77, v86, v87\n"
      "v_fma_f32 v91, v91, s48, v237\n"
      "v_exp_f32 v89, v89\n"
      "v_fma_f32 v92, v92, s48, v237\n"
      "v_exp_f32 v90, v90\n"
      "v_cvt_pk_bf16_f32 v78, v88, v89\n"
      "v_exp_f32 v91, v91\n"
      "v_exp_f32 v92, v92\n"
      "v_cvt_pk_bf16_f32 v79, v90, v91\n"
      "v_cvt_pk_bf16_f32 v154, v92, 0\n"
      "s_nop 0\n"
      "v_mfma_f32_32x32x16_bf16 v[116:131], v[150:153], v[76:79], v[116:131]\n"
      "v_rcp_f32 v236, v112\n"
      "s_nop 0\n"
      "v_mul_f32 v100, v100, v236\n"
      "v_mul_f32 v101, v101, v236\n"
      "v_cvt_pk_bf16_f32 v132, v100, v101\n"
      "v_mfma_f32_32x32x16_bf16 v[116:131], v[146:149], v[154:157], v[116:131]\n"
      "s_setprio 0\n"
      "ds_read_b32 v237, v253 offset:100\n"
      "v_mul_f32 v102, v102, v236\n"
      "v_mul_f32 v103, v103, v236\n"
      "v_cvt_pk_bf16_f32 v133, v102, v103\n"
      "v_mul_f32 v104, v104, v236\n"
      "v_mul_f32 v105, v105, v236\n"
      "v_cvt_pk_bf16_f32 v134, v104, v105\n"
      "v_mul_f32 v106, v106, v236\n"
      "v_mul_f32 v107, v107, v236\n"
      "v_cvt_pk_bf16_f32 v135, v106, v107\n"
      "v_mul_f32 v108, v108, v236\n"
      "v_mul_f32 v109, v109, v236\n"
      "v_cvt_pk_bf16_f32 v136, v108, v109\n"
      "v_mul_f32 v110, v110, v236\n"
      "v_mul_f32 v111, v111, v236\n"
      "v_cvt_pk_bf16_f32 v137, v110, v111\n"
      "v_add_f32 v60, v60, v239\n"
      "v_max3_f32 v238, v36, v37, v38\n"
      "v_max3_f32 v235, v39, v40, v41\n"
      "v_max3_f32 v234, v42, v43, v44\n"
      "v_max3_f32 v238, v238, v45, v46\n"
      "v_max3_f32 v235, v235, v47, v48\n"
      "v_max3_f32 v234, v234, v49, v50\n"
      "v_max3_f32 v238, v238, v51, v52\n"
      "v_max3_f32 v235, v235, v53, v54\n"
      "v_max3_f32 v234, v234, v55, v56\n"
      "v_max3_f32 v238, v238, v57, v58\n"
      "v_max3_f32 v235, v235, v59, v60\n"
      "v_max3_f32 v238, v238, v235, v234\n"
      "v_mov_b32 v235, v238\n"
      "s_nop 1\n"
      "v_permlane32_swap_b32 v238, v235\n"
      "v_max_f32 v238, v238, v235\n"
      "s_waitcnt lgkmcnt(0)\n"
      "v_readfirstlane_b32 s51, v237\n"
      "s_cmp_eq_u32 s51, 4\n"
      "s_cbranch_scc1 F24_%=\n"
      "F23_%=:\n"
      "ds_read_b32 v237, v253 offset:100\n"
      "s_waitcnt lgkmcnt(0)\n"
      "v_readfirstlane_b32 s51, v237\n"
      "s_cmp_eq_u32 s51, 4\n"
      "s_cbranch_scc1 F24_%=\n"
      "s_sleep 1\n"
      "s_sub_u32 s92, s92, 1\n"
      "s_cbranch_scc0 F23_%=\n"
      "F24_%=:\n"
      "ds_read_b64_tr_b16 v[190:191], v240 offset:0\n"
      "ds_read_b64_tr_b16 v[192:193], v240 offset:1536\n"
      "ds_read_b64_tr_b16 v[186:187], v240 offset:64\n"
      "ds_read_b64_tr_b16 v[188:189], v240 offset:1600\n"
      "ds_read_b64_tr_b16 v[112:113], v240 offset:128\n"
      "ds_read_b64_tr_b16 v[114:115], v240 offset:1664\n"
      "ds_read_b32 v237, v254 offset:64\n"
      "ds_read_b64_tr_b16 v[108:109], v240 offset:3072\n"
      "ds_read_b64_tr_b16 v[110:111], v240 offset:4608\n"
      "ds_read_b64_tr_b16 v[104:105], v240 offset:3136\n"
      "ds_read_b64_tr_b16 v[106:107], v240 offset:4672\n"
      "ds_read_b64_tr_b16 v[100:101], v240 offset:3200\n"
      "ds_read_b64_tr_b16 v[102:103], v240 offset:4736\n"
      "v_mul_f32 v238, 0xbfb8aa3b, v238\n"
      "v_fma_f32 v36, v36, s48, v238\n"
      "v_fma_f32 v37, v37, s48, v238\n"
      "v_fma_f32 v38, v38, s48, v238\n"
      "v_exp_f32 v36, v36\n"
      "v_fma_f32 v39, v39, s48, v238\n"
      "v_exp_f32 v37, v37\n"
      "v_fma_f32 v40, v40, s48, v238\n"
      "v_exp_f32 v38, v38\n"
      "v_cvt_pk_bf16_f32 v36, v36, v37\n"
      "v_fma_f32 v41, v41, s48, v238\n"
      "v_exp_f32 v39, v39\n"
      "v_fma_f32 v42, v42, s48, v238\n"
      "v_exp_f32 v40, v40\n"
      "v_cvt_pk_bf16_f32 v37, v38, v39\n"
      "v_fma_f32 v43, v43, s48, v238\n"
      "v_exp_f32 v41, v41\n"
      "v_fma_f32 v44, v44, s48, v238\n"
      "v_exp_f32 v42, v42\n"
      "v_cvt_pk_bf16_f32 v38, v40, v41\n"
      "v_fma_f32 v45, v45, s48, v238\n"
      "v_exp_f32 v43, v43\n"
      "v_fma_f32 v46, v46, s48, v238\n"
      "v_exp_f32 v44, v44\n"
      "v_rcp_f32 v236, v128\n"
      "s_nop 0\n"
      "v_mul_f32 v116, v116, v236\n"
      "v_mul_f32 v117, v117, v236\n"
      "v_cvt_pk_bf16_f32 v138, v116, v117\n"
      "v_mul_f32 v118, v118, v236\n"
      "v_mul_f32 v119, v119, v236\n"
      "v_cvt_pk_bf16_f32 v139, v118, v119\n"
      "v_mul_f32 v120, v120, v236\n"
      "v_mul_f32 v121, v121, v236\n"
      "v_cvt_pk_bf16_f32 v140, v120, v121\n"
      "v_mul_f32 v122, v122, v236\n"
      "v_mul_f32 v123, v123, v236\n"
      "v_cvt_pk_bf16_f32 v141, v122, v123\n"
      "v_mul_f32 v124, v124, v236\n"
      "v_mul_f32 v125, v125, v236\n"
      "v_cvt_pk_bf16_f32 v142, v124, v125\n"
      "v_mul_f32 v126, v126, v236\n"
      "v_mul_f32 v127, v127, v236\n"
      "v_cvt_pk_bf16_f32 v143, v126, v127\n"
      "s_mul_i32 s40, s35, 6144\n"
      "s_add_u32 s40, s40, s32\n"
      "v_lshlrev_b32 v235, 4, v1\n"
      "v_add_u32 v235, s40, v235\n"
      "s_mul_i32 s40, s25, 6144\n"
      "s_add_u32 s40, s40, s32\n"
      "v_lshlrev_b32 v239, 4, v1\n"
      "v_add_u32 v239, s40, v239\n"
      "ds_write_b128 v235, v[132:135] offset:0\n"
      "ds_write_b128 v235, v[136:139] offset:1024\n"
      "ds_write_b128 v235, v[140:143] offset:2048\n"
      "v_mov_b32 v236, 1\n"
      "ds_write_b32 v255, v236 offset:64\n"
      "s_waitcnt lgkmcnt(10)\n"
      "v_readfirstlane_b32 s51, v237\n"
      "s_cmp_lg_u32 s51, 0\n"
      "s_cbranch_scc1 F26_%=\n"
      "F25_%=:\n"
      "ds_read_b32 v237, v254 offset:64\n"
      "s_waitcnt lgkmcnt(0)\n"
      "v_readfirstlane_b32 s51, v237\n"
      "s_cmp_lg_u32 s51, 0\n"
      "s_cbranch_scc1 F26_%=\n"
      "s_sleep 1\n"
      "s_sub_u32 s92, s92, 1\n"
      "s_cbranch_scc0 F25_%=\n"
      "F26_%=:\n"
      "ds_read_b128 v[124:127], v239 offset:0\n"
      "ds_read_b128 v[120:123], v239 offset:1024\n"
      "ds_read_b128 v[116:119], v239 offset:2048\n"
      "v_cvt_pk_bf16_f32 v39, v42, v43\n"
      "v_fma_f32 v47, v47, s48, v238\n"
      "s_setprio 2\n"
      "v_mfma_f32_32x32x16_bf16 v[84:99], v[182:185], v[36:39], 0\n"
      "s_setprio 0\n"
      "v_exp_f32 v45, v45\n"
      "v_fma_f32 v48, v48, s48, v238\n"
      "v_exp_f32 v46, v46\n"
      "v_cvt_pk_bf16_f32 v40, v44, v45\n"
      "v_fma_f32 v49, v49, s48, v238\n"
      "v_exp_f32 v47, v47\n"
      "v_fma_f32 v50, v50, s48, v238\n"
      "v_exp_f32 v48, v48\n"
      "v_cvt_pk_bf16_f32 v41, v46, v47\n"
      "v_fma_f32 v51, v51, s48, v238\n"
      "v_exp_f32 v49, v49\n"
      "v_fma_f32 v52, v52, s48, v238\n"
      "v_exp_f32 v50, v50\n"
      "v_cvt_pk_bf16_f32 v42, v48, v49\n"
      "v_fma_f32 v53, v53, s48, v238\n"
      "v_exp_f32 v51, v51\n"
      "v_fma_f32 v54, v54, s48, v238\n"
      "v_exp_f32 v52, v52\n"
      "v_cvt_pk_bf16_f32 v43, v50, v51\n"
      "v_fma_f32 v55, v55, s48, v238\n"
      "s_setprio 2\n"
      "v_mfma_f32_32x32x16_bf16 v[84:99], v[174:177], v[40:43], v[84:99]\n"
      "s_setprio 0\n"
      "v_exp_f32 v53, v53\n"
      "v_fma_f32 v56, v56, s48, v238\n"
      "v_exp_f32 v54, v54\n"
      "v_cvt_pk_bf16_f32 v44, v52, v53\n"
      "v_fma_f32 v57, v57, s48, v238\n"
      "v_exp_f32 v55, v55\n"
      "v_fma_f32 v58, v58, s48, v238\n"
      "v_exp_f32 v56, v56\n"
      "v_cvt_pk_bf16_f32 v45, v54, v55\n"
      "v_fma_f32 v59, v59, s48, v238\n"
      "v_exp_f32 v57, v57\n"
      "v_fma_f32 v60, v60, s48, v238\n"
      "v_exp_f32 v58, v58\n"
      "v_cvt_pk_bf16_f32 v46, v56, v57\n"
      "v_exp_f32 v59, v59\n"
      "v_exp_f32 v60, v60\n"
      "v_cvt_pk_bf16_f32 v47, v58, v59\n"
      "v_cvt_pk_bf16_f32 v178, v60, 0\n"
      "s_setprio 2\n"
      "v_mfma_f32_32x32x16_bf16 v[84:99], v[170:173], v[44:47], v[84:99]\n"
      "v_add_f32 v28, v28, v241\n"
      "v_max3_f32 v233, v4, v5, v6\n"
      "v_max3_f32 v232, v7, v8, v9\n"
      "v_max3_f32 v231, v10, v11, v12\n"
      "v_max3_f32 v233, v233, v13, v14\n"
      "v_mfma_f32_32x32x16_bf16 v[84:99], v[166:169], v[178:181], v[84:99]\n"
      "v_max3_f32 v232, v232, v15, v16\n"
      "v_max3_f32 v231, v231, v17, v18\n"
      "v_max3_f32 v233, v233, v19, v20\n"
      "v_max3_f32 v232, v232, v21, v22\n"
      "v_max3_f32 v231, v231, v23, v24\n"
      "s_waitcnt lgkmcnt(2)\n"
      "v_mfma_f32_32x32x16_bf16 v[68:83], v[190:193], v[124:127], 0\n"
      "v_mul_u32_u24 v241, 192, v243\n"
      "v_add_u32 v241, s32, v241\n"
      "v_bfe_u32 v237, v243, 2, 2\n"
      "v_xor_b32 v237, v237, v3\n"
      "v_xor_b32 v238, 2, v237\n"
      "v_mfma_f32_32x32x16_bf16 v[52:67], v[186:189], v[124:127], 0\n"
      "v_lshl_add_u32 v238, v238, 4, v241\n"
      "v_lshl_add_u32 v241, v237, 4, v241\n"
      "s_mul_i32 s40, s25, 1536\n"
      "s_add_u32 s40, s40, s7\n"
      "s_add_u32 s40, s40, 154384\n"
      "s_mov_b32 s52, 0x1ffff\n"
      "s_mov_b32 s53, 0x1ffff\n"
      "s_cmp_lg_u32 s25, 0\n"
      "s_cselect_b64 s[52:53], s[52:53], -1\n"
      "s_cmp_eq_u32 s31, 0\n"
      "s_cselect_b32 s41, s28, 0\n"
      "s_add_u32 s38, s20, s41\n"
      "s_addc_u32 s39, s21, 0\n"
      "v_lshlrev_b32 v230, 2, v1\n"
      "v_add_u32 v230, s40, v230\n"
      "v_mfma_f32_32x32x16_bf16 v[36:51], v[112:115], v[124:127], 0\n"
      "ds_read_b64_tr_b16 v[136:137], v240 offset:6144\n"
      "ds_read_b64_tr_b16 v[138:139], v240 offset:7680\n"
      "ds_read_b64_tr_b16 v[132:133], v240 offset:6208\n"
      "ds_read_b64_tr_b16 v[134:135], v240 offset:7744\n"
      "ds_read_b64_tr_b16 v[128:129], v240 offset:6272\n"
      "ds_read_b64_tr_b16 v[130:131], v240 offset:7808\n"
      "s_waitcnt lgkmcnt(7)\n"
      "v_mfma_f32_32x32x16_bf16 v[68:83], v[108:111], v[120:123], v[68:83]\n"
      "ds_read_b32 v243, v230 offset:0\n"
      "ds_read_b32 v237, v230 offset:256\n"
      "ds_read_b32 v236, v230 offset:512\n"
      "ds_read_b32 v235, v230 offset:768\n"
      "ds_read_b32 v234, v230 offset:1024\n"
      "v_mfma_f32_32x32x16_bf16 v[52:67], v[104:107], v[120:123], v[52:67]\n"
      "ds_read_b32 v240, v230 offset:1280\n"
      "v_max3_f32 v233, v233, v25, v26\n"
      "v_max3_f32 v232, v232, v27, v28\n"
      "v_max3_f32 v233, v233, v232, v231\n"
      "v_mov_b32 v232, v233\n"
      "v_mfma_f32_32x32x16_bf16 v[36:51], v[100:103], v[120:123], v[36:51]\n"
      "s_nop 0\n"
      "v_permlane32_swap_b32 v233, v232\n"
      "v_max_f32 v233, v233, v232\n"
      "v_mul_f32 v233, 0xbfb8aa3b, v233\n"
      "v_fma_f32 v4, v4, s48, v233\n"
      "v_fma_f32 v5, v5, s48, v233\n"
      "v_fma_f32 v6, v6, s48, v233\n"
      "v_exp_f32 v4, v4\n"
      "v_fma_f32 v7, v7, s48, v233\n"
      "v_exp_f32 v5, v5\n"
      "s_waitcnt lgkmcnt(10)\n"
      "v_mfma_f32_32x32x16_bf16 v[68:83], v[136:139], v[116:119], v[68:83]\n"
      "v_fma_f32 v8, v8, s48, v233\n"
      "v_exp_f32 v6, v6\n"
      "v_cvt_pk_bf16_f32 v4, v4, v5\n"
      "v_fma_f32 v9, v9, s48, v233\n"
      "s_waitcnt lgkmcnt(8)\n"
      "v_mfma_f32_32x32x16_bf16 v[52:67], v[132:135], v[116:119], v[52:67]\n"
      "v_exp_f32 v7, v7\n"
      "v_fma_f32 v10, v10, s48, v233\n"
      "v_exp_f32 v8, v8\n"
      "s_waitcnt lgkmcnt(6)\n"
      "v_mfma_f32_32x32x16_bf16 v[36:51], v[128:131], v[116:119], v[36:51]\n"
      "s_setprio 0\n"
      "ds_read_b64_tr_b16 v[120:121], v242 offset:0\n"
      "ds_read_b64_tr_b16 v[122:123], v242 offset:1536\n"
      "ds_read_b64_tr_b16 v[112:113], v242 offset:64\n"
      "ds_read_b64_tr_b16 v[114:115], v242 offset:1600\n"
      "ds_read_b64_tr_b16 v[124:125], v242 offset:128\n"
      "ds_read_b64_tr_b16 v[126:127], v242 offset:1664\n"
      "v_rcp_f32 v232, v96\n"
      "s_nop 0\n"
      "v_mul_f32 v84, v84, v232\n"
      "v_mul_f32 v85, v85, v232\n"
      "v_cvt_pk_bf16_f32 v100, v84, v85\n"
      "v_mul_f32 v86, v86, v232\n"
      "v_mul_f32 v87, v87, v232\n"
      "v_cvt_pk_bf16_f32 v101, v86, v87\n"
      "v_mul_f32 v88, v88, v232\n"
      "v_mul_f32 v89, v89, v232\n"
      "v_cvt_pk_bf16_f32 v102, v88, v89\n"
      "v_mul_f32 v90, v90, v232\n"
      "v_mul_f32 v91, v91, v232\n"
      "v_cvt_pk_bf16_f32 v103, v90, v91\n"
      "v_mul_f32 v92, v92, v232\n"
      "v_mul_f32 v93, v93, v232\n"
      "v_cvt_pk_bf16_f32 v104, v92, v93\n"
      "v_mul_f32 v94, v94, v232\n"
      "v_mul_f32 v95, v95, v232\n"
      "v_cvt_pk_bf16_f32 v105, v94, v95\n"
      "v_cvt_pk_bf16_f32 v5, v6, v7\n"
      "v_fma_f32 v11, v11, s48, v233\n"
      "v_exp_f32 v9, v9\n"
      "v_fma_f32 v12, v12, s48, v233\n"
      "v_exp_f32 v10, v10\n"
      "v_cvt_pk_bf16_f32 v6, v8, v9\n"
      "v_fma_f32 v13, v13, s48, v233\n"
      "s_setprio 2\n"
      "s_waitcnt lgkmcnt(4)\n"
      "v_mfma_f32_32x32x16_bf16 v[68:83], v[120:123], v[100:103], v[68:83]\n"
      "v_exp_f32 v11, v11\n"
      "v_fma_f32 v14, v14, s48, v233\n"
      "v_exp_f32 v12, v12\n"
      "s_waitcnt lgkmcnt(2)\n"
      "v_mfma_f32_32x32x16_bf16 v[52:67], v[112:115], v[100:103], v[52:67]\n"
      "v_cvt_pk_bf16_f32 v7, v10, v11\n"
      "v_fma_f32 v15, v15, s48, v233\n"
      "v_exp_f32 v13, v13\n"
      "v_fma_f32 v16, v16, s48, v233\n"
      "s_waitcnt lgkmcnt(0)\n"
      "v_mfma_f32_32x32x16_bf16 v[36:51], v[124:127], v[100:103], v[36:51]\n"
      "ds_read_b64_tr_b16 v[120:121], v242 offset:3072\n"
      "ds_read_b64_tr_b16 v[122:123], v242 offset:4608\n"
      "ds_read_b64_tr_b16 v[116:117], v242 offset:3136\n"
      "ds_read_b64_tr_b16 v[118:119], v242 offset:4672\n"
      "ds_read_b64_tr_b16 v[112:113], v242 offset:3200\n"
      "ds_read_b64_tr_b16 v[114:115], v242 offset:4736\n"
      "v_mfma_f32_32x32x16_bf16 v[84:99], v[162:165], v[4:7], 0\n"
      "s_setprio 0\n"
      "v_exp_f32 v14, v14\n"
      "v_cvt_pk_bf16_f32 v8, v12, v13\n"
      "v_fma_f32 v17, v17, s48, v233\n"
      "v_exp_f32 v15, v15\n"
      "v_fma_f32 v18, v18, s48, v233\n"
      "v_exp_f32 v16, v16\n"
      "v_cvt_pk_bf16_f32 v9, v14, v15\n"
      "v_fma_f32 v19, v19, s48, v233\n"
      "v_exp_f32 v17, v17\n"
      "v_fma_f32 v20, v20, s48, v233\n"
      "v_exp_f32 v18, v18\n"
      "v_cvt_pk_bf16_f32 v10, v16, v17\n"
      "v_fma_f32 v21, v21, s48, v233\n"
      "v_exp_f32 v19, v19\n"
      "v_fma_f32 v22, v22, s48, v233\n"
      "v_exp_f32 v20, v20\n"
      "s_cmp_eq_u32 s31, 0\n"
      "s_cbranch_scc1 Wf27_%=\n"
      "v_bfe_i32 v232, v243, 0, 1\n"
      "v_bfe_i32 v231, v243, 1, 1\n"
      "v_and_b32 v232, s29, v232\n"
      "v_and_b32 v231, s30, v231\n"
      "v_add_u32 v243, s28, v243\n"
      "v_sub_u32 v243, v243, v232\n"
      "v_sub_u32 v243, v243, v231\n"
      "v_bfe_i32 v232, v237, 0, 1\n"
      "v_bfe_i32 v231, v237, 1, 1\n"
      "v_and_b32 v232, s29, v232\n"
      "v_and_b32 v231, s30, v231\n"
      "v_add_u32 v237, s28, v237\n"
      "v_sub_u32 v237, v237, v232\n"
      "v_sub_u32 v237, v237, v231\n"
      "v_bfe_i32 v232, v236, 0, 1\n"
      "v_bfe_i32 v231, v236, 1, 1\n"
      "v_and_b32 v232, s29, v232\n"
      "v_and_b32 v231, s30, v231\n"
      "v_add_u32 v236, s28, v236\n"
      "v_sub_u32 v236, v236, v232\n"
      "v_sub_u32 v236, v236, v231\n"
      "v_bfe_i32 v232, v235, 0, 1\n"
      "v_bfe_i32 v231, v235, 1, 1\n"
      "v_and_b32 v232, s29, v232\n"
      "v_and_b32 v231, s30, v231\n"
      "v_add_u32 v235, s28, v235\n"
      "v_sub_u32 v235, v235, v232\n"
      "v_sub_u32 v235, v235, v231\n"
      "v_bfe_i32 v232, v234, 0, 1\n"
      "v_bfe_i32 v231, v234, 1, 1\n"
      "v_and_b32 v232, s29, v232\n"
      "v_and_b32 v231, s30, v231\n"
      "v_add_u32 v234, s28, v234\n"
      "v_sub_u32 v234, v234, v232\n"
      "v_sub_u32 v234, v234, v231\n"
      "v_bfe_i32 v232, v240, 0, 1\n"
      "v_bfe_i32 v231, v240, 1, 1\n"
      "v_and_b32 v232, s29, v232\n"
      "v_and_b32 v231, s30, v231\n"
      "v_add_u32 v240, s28, v240\n"
      "v_sub_u32 v240, v240, v232\n"
      "v_sub_u32 v240, v240, v231\n"
      "Wf27_%=:\n"
      "v_and_b32 v243, -16, v243\n"
      "v_and_b32 v237, -16, v237\n"
      "v_and_b32 v236, -16, v236\n"
      "v_and_b32 v235, -16, v235\n"
      "v_and_b32 v234, -16, v234\n"
      "v_and_b32 v240, -16, v240\n"
      "v_cvt_pk_bf16_f32 v11, v18, v19\n"
      "v_fma_f32 v23, v23, s48, v233\n"
      "s_setprio 2\n"
      "v_mfma_f32_32x32x16_bf16 v[84:99], v[158:161], v[8:11], v[84:99]\n"
      "s_setprio 0\n"
      "v_exp_f32 v21, v21\n"
      "v_fma_f32 v24, v24, s48, v233\n"
      "v_exp_f32 v22, v22\n"
      "v_cvt_pk_bf16_f32 v12, v20, v21\n"
      "v_fma_f32 v25, v25, s48, v233\n"
      "v_exp_f32 v23, v23\n"
      "v_fma_f32 v26, v26, s48, v233\n"
      "v_exp_f32 v24, v24\n"
      "v_cvt_pk_bf16_f32 v13, v22, v23\n"
      "v_fma_f32 v27, v27, s48, v233\n"
      "v_exp_f32 v25, v25\n"
      "v_fma_f32 v28, v28, s48, v233\n"
      "v_exp_f32 v26, v26\n"
      "v_cvt_pk_bf16_f32 v14, v24, v25\n"
      "v_exp_f32 v27, v27\n"
      "v_exp_f32 v28, v28\n"
      "v_cvt_pk_bf16_f32 v15, v26, v27\n"
      "v_cvt_pk_bf16_f32 v154, v28, 0\n"
      "s_setprio 2\n"
      "v_mfma_f32_32x32x16_bf16 v[84:99], v[150:153], v[12:15], v[84:99]\n"
      "v_mfma_f32_32x32x16_bf16 v[84:99], v[146:149], v[154:157], v[84:99]\n"
      "s_setprio 0\n"
      "ds_read_b64_tr_b16 v[124:125], v242 offset:6144\n"
      "ds_read_b64_tr_b16 v[126:127], v242 offset:7680\n"
      "ds_read_b64_tr_b16 v[128:129], v242 offset:6208\n"
      "ds_read_b64_tr_b16 v[130:131], v242 offset:7744\n"
      "ds_read_b64_tr_b16 v[20:21], v242 offset:6272\n"
      "ds_read_b64_tr_b16 v[22:23], v242 offset:7808\n"
      "s_nop 4\n"
      "v_rcp_f32 v233, v96\n"
      "s_nop 0\n"
      "v_mul_f32 v84, v84, v233\n"
      "v_mul_f32 v85, v85, v233\n"
      "v_cvt_pk_bf16_f32 v106, v84, v85\n"
      "v_mul_f32 v86, v86, v233\n"
      "v_mul_f32 v87, v87, v233\n"
      "v_cvt_pk_bf16_f32 v107, v86, v87\n"
      "v_mul_f32 v88, v88, v233\n"
      "s_setprio 2\n"
      "s_waitcnt lgkmcnt(10)\n"
      "v_mfma_f32_32x32x16_bf16 v[68:83], v[120:123], v[104:107], v[68:83]\n"
      "v_mul_f32 v89, v89, v233\n"
      "v_cvt_pk_bf16_f32 v108, v88, v89\n"
      "v_mul_f32 v90, v90, v233\n"
      "v_mul_f32 v91, v91, v233\n"
      "v_cvt_pk_bf16_f32 v109, v90, v91\n"
      "s_waitcnt lgkmcnt(8)\n"
      "v_mfma_f32_32x32x16_bf16 v[52:67], v[116:119], v[104:107], v[52:67]\n"
      "v_mul_f32 v92, v92, v233\n"
      "v_mul_f32 v93, v93, v233\n"
      "v_cvt_pk_bf16_f32 v110, v92, v93\n"
      "v_mul_f32 v94, v94, v233\n"
      "v_mul_f32 v95, v95, v233\n"
      "s_waitcnt lgkmcnt(6)\n"
      "v_mfma_f32_32x32x16_bf16 v[36:51], v[112:115], v[104:107], v[36:51]\n"
      "v_cvt_pk_bf16_f32 v111, v94, v95\n"
      "s_waitcnt lgkmcnt(4)\n"
      "s_nop 0\n"
      "v_mfma_f32_32x32x16_bf16 v[68:83], v[124:127], v[108:111], v[68:83]\n"
      "s_waitcnt lgkmcnt(2)\n"
      "v_mfma_f32_32x32x16_bf16 v[52:67], v[128:131], v[108:111], v[52:67]\n"
      "s_waitcnt lgkmcnt(0)\n"
      "v_mfma_f32_32x32x16_bf16 v[36:51], v[20:23], v[108:111], v[36:51]\n"
      "s_setprio 0\n"
      "s_nop 6\n"
      "v_cvt_pk_bf16_f32 v4, v68, v69\n"
      "v_cvt_pk_bf16_f32 v5, v70, v71\n"
      "v_cvt_pk_bf16_f32 v6, v72, v73\n"
      "v_cvt_pk_bf16_f32 v7, v74, v75\n"
      "v_cvt_pk_bf16_f32 v8, v76, v77\n"
      "v_cvt_pk_bf16_f32 v9, v78, v79\n"
      "v_cvt_pk_bf16_f32 v10, v80, v81\n"
      "v_cvt_pk_bf16_f32 v11, v82, v83\n"
      "v_cvt_pk_bf16_f32 v12, v52, v53\n"
      "v_cvt_pk_bf16_f32 v13, v54, v55\n"
      "v_cvt_pk_bf16_f32 v14, v56, v57\n"
      "v_cvt_pk_bf16_f32 v15, v58, v59\n"
      "v_cvt_pk_bf16_f32 v16, v60, v61\n"
      "v_cvt_pk_bf16_f32 v17, v62, v63\n"
      "v_cvt_pk_bf16_f32 v18, v64, v65\n"
      "v_cvt_pk_bf16_f32 v19, v66, v67\n"
      "v_cvt_pk_bf16_f32 v20, v36, v37\n"
      "v_cvt_pk_bf16_f32 v21, v38, v39\n"
      "v_cvt_pk_bf16_f32 v22, v40, v41\n"
      "v_cvt_pk_bf16_f32 v23, v42, v43\n"
      "v_cvt_pk_bf16_f32 v24, v44, v45\n"
      "v_cvt_pk_bf16_f32 v25, v46, v47\n"
      "v_cvt_pk_bf16_f32 v26, v48, v49\n"
      "v_cvt_pk_bf16_f32 v27, v50, v51\n"
      "s_mov_b64 exec, s[52:53]\n"
      "ds_write_b128 v241, v[4:7] offset:0\n"
      "ds_write_b128 v238, v[8:11] offset:0\n"
      "ds_write_b128 v241, v[12:15] offset:64\n"
      "ds_write_b128 v238, v[16:19] offset:64\n"
      "ds_write_b128 v241, v[20:23] offset:128\n"
      "ds_write_b128 v238, v[24:27] offset:128\n"
      "s_mov_b64 exec, -1\n"
      "ds_read_b128 v[4:7], v239 offset:0\n"
      "ds_read_b128 v[8:11], v239 offset:1024\n"
      "ds_read_b128 v[12:15], v239 offset:2048\n"
      "ds_read_b128 v[16:19], v239 offset:3072\n"
      "ds_read_b128 v[20:23], v239 offset:4096\n"
      "ds_read_b128 v[24:27], v239 offset:5120\n"
      "s_waitcnt lgkmcnt(5)\n"
      "global_store_dwordx4 v243, v[4:7], s[38:39] sc1\n"
      "s_waitcnt lgkmcnt(4)\n"
      "global_store_dwordx4 v237, v[8:11], s[38:39] sc1\n"
      "s_waitcnt lgkmcnt(3)\n"
      "global_store_dwordx4 v236, v[12:15], s[38:39] sc1\n"
      "s_waitcnt lgkmcnt(2)\n"
      "s_mov_b64 exec, s[46:47]\n"
      "global_store_dwordx4 v235, v[16:19], s[38:39] sc1\n"
      "s_waitcnt lgkmcnt(1)\n"
      "s_cmp_lg_u32 s25, 0\n"
      "s_cbranch_scc1 End28_%=\n"
      "global_store_dwordx4 v234, v[20:23], s[38:39] sc1\n"
      "s_waitcnt lgkmcnt(0)\n"
      "global_store_dwordx4 v240, v[24:27], s[38:39] sc1\n"
      "End28_%=:\n"
      :
      : "{v0}"(threadIdx.x), "{s[4:5]}"(__builtin_amdgcn_kernarg_segment_ptr()), "{s6}"(blockIdx.x), "{s7}"(sb)
      : "memory", "vcc", "scc", "s8", "s9", "s10", "s11", "s12", "s13", "s14", "s15", "s16", "s17", "s18", "s19", "s20", "s21", "s22", "s23", "s24", "s25", "s26", "s27", "s28", "s29", "s30", "s31", "s32", "s33", "s34", "s35", "s36", "s37", "s38", "s39", "s40", "s41", "s42", "s43", "s44", "s45", "s46", "s47", "s48", "s49", "s50", "s51", "s52", "s53", "s54", "s55", "s56", "s57", "s58", "s59", "s60", "s61", "s62", "s63", "s64", "s65", "s66", "s67", "s68", "s69", "s70", "s71", "s72", "s73", "s74", "s75", "s76", "s77", "s78", "s79", "s80", "s81", "s82", "s83", "s84", "s85", "s86", "s87", "s88", "s89", "s90", "s91", "s92", "s93", "s94", "s95",
        "v1", "v2", "v3", "v4", "v5", "v6", "v7", "v8", "v9", "v10", "v11", "v12", "v13", "v14", "v15", "v16", "v17", "v18", "v19", "v20", "v21", "v22", "v23", "v24", "v25", "v26", "v27", "v28", "v29", "v30", "v31", "v32", "v33", "v34", "v35", "v36", "v37", "v38", "v39", "v40", "v41", "v42", "v43", "v44", "v45", "v46", "v47", "v48", "v49", "v50", "v51", "v52", "v53", "v54", "v55", "v56", "v57", "v58", "v59", "v60", "v61", "v62", "v63", "v64", "v65", "v66", "v67", "v68", "v69", "v70", "v71", "v72", "v73", "v74", "v75", "v76", "v77", "v78", "v79", "v80", "v81", "v82", "v83", "v84", "v85", "v86", "v87", "v88", "v89", "v90", "v91", "v92", "v93", "v94", "v95", "v96", "v97", "v98", "v99", "v100", "v101", "v102", "v103", "v104", "v105", "v106", "v107", "v108", "v109", "v110", "v111", "v112", "v113", "v114", "v115", "v116", "v117", "v118", "v119", "v120", "v121", "v122", "v123", "v124", "v125", "v126", "v127", "v128", "v129", "v130", "v131", "v132", "v133", "v134", "v135", "v136", "v137", "v138", "v139", "v140", "v141", "v142", "v143", "v144", "v145", "v146", "v147", "v148", "v149", "v150", "v151", "v152", "v153", "v154", "v155", "v156", "v157", "v158", "v159", "v160", "v161", "v162", "v163", "v164", "v165", "v166", "v167", "v168", "v169", "v170", "v171", "v172", "v173", "v174", "v175", "v176", "v177", "v178", "v179", "v180", "v181", "v182", "v183", "v184", "v185", "v186", "v187", "v188", "v189", "v190", "v191", "v192", "v193", "v194", "v195", "v196", "v197", "v198", "v199", "v200", "v201", "v202", "v203", "v204", "v205", "v206", "v207", "v208", "v209", "v210", "v211", "v212", "v213", "v214", "v215", "v216", "v217", "v218", "v219", "v220", "v221", "v222", "v223", "v224", "v225", "v226", "v227", "v228", "v229", "v230", "v231", "v232", "v233", "v234", "v235", "v236", "v237", "v238", "v239", "v240", "v241", "v242", "v243", "v244", "v245", "v246", "v247", "v248", "v249", "v250", "v251", "v252", "v253", "v254", "v255");
#endif
}

extern "C" void kernel_launch(void* const* d_in, const int* in_sizes, int n_in, void* d_out, int out_size, void* d_ws,
                              size_t ws_size, hipStream_t stream) {
  swin_attn_kernel<<<256, 512, 0, stream>>>((const unsigned short*)d_in[0], (const unsigned short*)d_in[1],
                                            (const unsigned short*)d_in[2], (const unsigned short*)d_in[3],
                                            (const int*)d_in[4], (const unsigned short*)d_in[5], (unsigned short*)d_out);
}
```
